```python
import torch

def _softmax(x, axis=-1):
    return torch.softmax(x, dim=axis)

def _gelu(x):
    return torch.nn.functional.gelu(x, approximate='tanh')

def _silu(x):
    return torch.nn.functional.silu(x)

def _sigmoid(x):
    return torch.sigmoid(x)

def _layernorm(x, axis=-1, eps=1e-05):
    m = x.mean(dim=axis, keepdim=True)
    v = x.var(dim=axis, keepdim=True, unbiased=False)
    return (x - m) / torch.sqrt(v + eps)

def _scatter_add(tgt, idx, src, axis=0):
    return tgt.clone().index_add_(axis, idx, src)

def _segment_sum(src, seg, n):
    out = torch.zeros((n,) + src.shape[1:], dtype=src.dtype)
    return out.index_add_(0, seg, src)

def _pad(x, pw):
    flat = tuple((v for pair in reversed(pw) for v in pair))
    return torch.nn.functional.pad(x, flat)
kernel_name = 'standalone__conformer_family_zipformer__speech'

def setup_inputs():

    _w = lambda a: (lambda t: t.to(torch.bfloat16) if t.is_floating_point() else t)(torch.as_tensor(a))
    B, T, D, K, H = (2, 1000, 256, 31, 4)
    sc = 0.1
    return {'x': _w((lambda *s: torch.randn(*s, dtype=torch.bfloat16) * 0.8)(B, T, D)), 'w_ff1a': _w((lambda *s: torch.randn(*s, dtype=torch.bfloat16) * 0.8)(D, 4 * D) * sc), 'w_ff1b': _w((lambda *s: torch.randn(*s, dtype=torch.bfloat16) * 0.8)(4 * D, D) * sc), 'w_qkv': _w((lambda *s: torch.randn(*s, dtype=torch.bfloat16) * 0.8)(D, 3 * D) * sc), 'w_o': _w((lambda *s: torch.randn(*s, dtype=torch.bfloat16) * 1.6)(D, D) * sc), 'w_pw1': _w((lambda *s: torch.randn(*s, dtype=torch.bfloat16) * 1.6)(D, 2 * D) * sc), 'dw': _w((lambda *s: torch.randn(*s, dtype=torch.bfloat16) * 0.4)(K, D)), 'w_pw2': _w((lambda *s: torch.randn(*s, dtype=torch.bfloat16) * 0.4)(D, D) * sc), 'w_ff2a': _w((lambda *s: torch.randn(*s, dtype=torch.bfloat16) * 0.8)(D, 4 * D) * sc), 'w_ff2b': _w((lambda *s: torch.randn(*s, dtype=torch.bfloat16) * 0.8)(4 * D, D) * sc)}

def reference(x, w_ff1a, w_ff1b, w_qkv, w_o, w_pw1, dw, w_pw2, w_ff2a, w_ff2b):
    B, T, D = x.shape
    K = dw.shape[0]
    H = 4
    Dh = D // H
    h = _layernorm(x, axis=-1)
    h = _silu(h @ w_ff1a) @ w_ff1b
    x = x + 0.5 * h
    h = _layernorm(x, axis=-1)
    qkv = h @ w_qkv
    q = (lambda x, *d: x.permute(*d))(qkv[..., :D].reshape(B, T, H, Dh), 0, 2, 1, 3)
    k = (lambda x, *d: x.permute(*d))(qkv[..., D:2 * D].reshape(B, T, H, Dh), 0, 2, 1, 3)
    v = (lambda x, *d: x.permute(*d))(qkv[..., 2 * D:].reshape(B, T, H, Dh), 0, 2, 1, 3)
    scale = Dh ** (-0.5)
    scores = torch.einsum('bhqd,bhkd->bhqk', q, k) * scale
    p = _softmax(scores, axis=-1)
    att = torch.einsum('bhqk,bhkd->bhqd', p, v)
    att = (lambda x, *d: x.permute(*d))(att, 0, 2, 1, 3).reshape(B, T, D)
    x = x + att @ w_o
    h = _layernorm(x, axis=-1)
    h = h @ w_pw1
    a, b = (h[..., :D].clone(), h[..., D:].clone())
    h = a * _sigmoid(b)
    pad = K // 2
    hp = _pad(h, ((0, 0), (pad, pad), (0, 0)))
    c = torch.zeros_like(h)
    for i in range(K):
        c = c + hp[:, i:i + T, :] * dw[i][None, None, :]
    x = x + _silu(c) @ w_pw2
    h = _layernorm(x, axis=-1)
    h = _silu(h @ w_ff2a) @ w_ff2b
    x = x + 0.5 * h
    return _layernorm(x, axis=-1)
```

```cpp
#include <hip/hip_runtime.h>
#include <hip/hip_bf16.h>
typedef unsigned short u16;
__device__ __forceinline__ float b2f(u16 v){ return __uint_as_float(((unsigned)v)<<16); }
__device__ __forceinline__ u16 f2b(float f){ unsigned u=__float_as_uint(f); u += 0x7FFF + ((u>>16)&1); return (u16)(u>>16); }
__global__ void n_cvt(const u16* x, float* y, int n){ int i=blockIdx.x*256+threadIdx.x; if(i<n) y[i]=b2f(x[i]); }
__global__ void n_ln(const float* x, float* y, u16* yb){
  __shared__ float s[256]; int r=blockIdx.x, t=threadIdx.x; float v=x[r*256+t];
  s[t]=v; __syncthreads(); for(int o=128;o>0;o>>=1){ if(t<o) s[t]+=s[t+o]; __syncthreads(); } float m=s[0]/256.f; __syncthreads();
  float d=v-m; s[t]=d*d; __syncthreads(); for(int o=128;o>0;o>>=1){ if(t<o) s[t]+=s[t+o]; __syncthreads(); } float var=s[0]/256.f;
  float o=d*rsqrtf(var+1e-5f); if(y) y[r*256+t]=o; if(yb) yb[r*256+t]=f2b(o);
}
__global__ void n_gemm(const float* A, const u16* W, float* C, const float* res, float alpha, int M,int N,int K,int mode){
  int n=blockIdx.x*64+(threadIdx.x&63); int m=blockIdx.y*4+(threadIdx.x>>6); if(m>=M||n>=N) return;
  float acc=0; for(int k=0;k<K;k++) acc+=A[m*K+k]*b2f(W[k*N+n]);
  if(mode==1) acc=acc/(1.f+__expf(-acc)); if(res) acc=res[m*N+n]+alpha*acc; C[m*N+n]=acc;
}
__global__ void n_attn(const float* qkv, float* att){
  __shared__ float p[1000]; __shared__ float red[64];
  int row=blockIdx.x, h=blockIdx.y, t=threadIdx.x; int b=row/1000;
  const float* q=qkv+row*768+h*64; float mx=-1e30f;
  for(int k=t;k<1000;k+=64){ const float* kk=qkv+(b*1000+k)*768+256+h*64; float s=0; for(int d=0;d<64;d++) s+=q[d]*kk[d]; s*=0.125f; p[k]=s; mx=fmaxf(mx,s);}
  red[t]=mx; __syncthreads(); for(int o=32;o>0;o>>=1){ if(t<o) red[t]=fmaxf(red[t],red[t+o]); __syncthreads(); } mx=red[0]; __syncthreads();
  float sum=0; for(int k=t;k<1000;k+=64){ float e=__expf(p[k]-mx); p[k]=e; sum+=e; }
  red[t]=sum; __syncthreads(); for(int o=32;o>0;o>>=1){ if(t<o) red[t]+=red[t+o]; __syncthreads(); } sum=red[0];
  float o=0; for(int k=0;k<1000;k++) o+=p[k]*qkv[(b*1000+k)*768+512+h*64+t];
  att[row*256+h*64+t]=o/sum;
}
__global__ void n_glu(const float* h, float* g){ int r=blockIdx.x,t=threadIdx.x; float a=h[r*512+t], b=h[r*512+256+t]; g[r*256+t]=a/(1.f+__expf(-b)); }
__global__ void n_conv(const float* g, const u16* dw, float* c){ int r=blockIdx.x,t=threadIdx.x; int b=r/1000, tt=r%1000; float acc=0;
  for(int i=0;i<31;i++){ int s=tt+i-15; if(s>=0&&s<1000) acc+=g[(b*1000+s)*256+t]*b2f(dw[i*256+t]); }
  c[r*256+t]=acc/(1.f+__expf(-acc)); }
struct NaiveBufs { float *x0,*ln,*hid,*x1,*qkv,*att,*x2,*pw1,*glu,*cv,*x3,*x4; };
static void naive_run(void* const* d_in, void* d_out, float* ws, hipStream_t s, NaiveBufs* nb){
  const u16* x=(const u16*)d_in[0]; const u16* w1a=(const u16*)d_in[1]; const u16* w1b=(const u16*)d_in[2]; const u16* wqkv=(const u16*)d_in[3];
  const u16* wo=(const u16*)d_in[4]; const u16* wp1=(const u16*)d_in[5]; const u16* dw=(const u16*)d_in[6]; const u16* wp2=(const u16*)d_in[7];
  const u16* w2a=(const u16*)d_in[8]; const u16* w2b=(const u16*)d_in[9];
  const int M=2000; NaiveBufs b; float* p=ws;
  b.x0=p; p+=M*256; b.ln=p; p+=M*256; b.hid=p; p+=M*1024; b.x1=p; p+=M*256; b.qkv=p; p+=M*768; b.att=p; p+=M*256; b.x2=p; p+=M*256; b.pw1=p; p+=M*512; b.glu=p; p+=M*256; b.cv=p; p+=M*256; b.x3=p; p+=M*256; b.x4=p; p+=M*256;
  n_cvt<<<M,256,0,s>>>(x,b.x0,M*256);
  n_ln<<<M,256,0,s>>>(b.x0,b.ln,nullptr);
  n_gemm<<<dim3(16,M/4),256,0,s>>>(b.ln,w1a,b.hid,nullptr,0,M,1024,256,1);
  n_gemm<<<dim3(4,M/4),256,0,s>>>(b.hid,w1b,b.x1,b.x0,0.5f,M,256,1024,0);
  n_ln<<<M,256,0,s>>>(b.x1,b.ln,nullptr);
  n_gemm<<<dim3(12,M/4),256,0,s>>>(b.ln,wqkv,b.qkv,nullptr,0,M,768,256,0);
  n_attn<<<dim3(M,4),64,0,s>>>(b.qkv,b.att);
  n_gemm<<<dim3(4,M/4),256,0,s>>>(b.att,wo,b.x2,b.x1,1.f,M,256,256,0);
  n_ln<<<M,256,0,s>>>(b.x2,b.ln,nullptr);
  n_gemm<<<dim3(8,M/4),256,0,s>>>(b.ln,wp1,b.pw1,nullptr,0,M,512,256,0);
  n_glu<<<M,256,0,s>>>(b.pw1,b.glu);
  n_conv<<<M,256,0,s>>>(b.glu,dw,b.cv);
  n_gemm<<<dim3(4,M/4),256,0,s>>>(b.cv,wp2,b.x3,b.x2,1.f,M,256,256,0);
  n_ln<<<M,256,0,s>>>(b.x3,b.ln,nullptr);
  n_gemm<<<dim3(16,M/4),256,0,s>>>(b.ln,w2a,b.hid,nullptr,0,M,1024,256,1);
  n_gemm<<<dim3(4,M/4),256,0,s>>>(b.hid,w2b,b.x4,b.x3,0.5f,M,256,1024,0);
  n_ln<<<M,256,0,s>>>(b.x4,nullptr,(u16*)d_out);
  if(nb) *nb=b;
}
extern "C" void kernel_launch(void* const* d_in, const int* in_sizes, int n_in, void* d_out, int out_size, void* d_ws, size_t ws_size, hipStream_t stream){
  naive_run(d_in,d_out,(float*)d_ws,stream,nullptr);
}
```

```cpp
#include <hip/hip_runtime.h>
#include <hip/hip_bf16.h>
#include <stdint.h>

typedef unsigned short u16;
typedef unsigned long long u64;
typedef float f32x16 __attribute__((ext_vector_type(16)));
typedef float f32x4 __attribute__((ext_vector_type(4)));
typedef float f32x2 __attribute__((ext_vector_type(2)));
typedef __bf16 bf16x8 __attribute__((ext_vector_type(8)));
typedef __bf16 bf16x2 __attribute__((ext_vector_type(2)));
typedef short s16x4 __attribute__((ext_vector_type(4)));
typedef unsigned u32x4 __attribute__((ext_vector_type(4)));
typedef unsigned u32x2 __attribute__((ext_vector_type(2)));
#define LDSP(T, off) ((__attribute__((address_space(3))) T*)(uintptr_t)(off))
#define GAS __attribute__((address_space(1)))
#define DEV __device__ __forceinline__

#define MAPB 0

constexpr unsigned OFF_EPOCH = 0;
constexpr unsigned OFF_FLAG = 4096;
constexpr unsigned OFF_P1 = 1u << 20;
constexpr unsigned OFF_KV = OFF_P1 + (4u << 20);
constexpr unsigned OFF_ATT = OFF_KV + (2u << 20);
constexpr unsigned OFF_GLU = OFF_ATT + (1u << 20);
constexpr unsigned OFF_P5 = OFF_GLU + (1u << 20);
constexpr unsigned WS_USED = OFF_P5 + (4u << 20);
constexpr unsigned OFF_PROF = WS_USED;
constexpr unsigned BUFA = 131072, BUFB = 131072 + 16384;
constexpr unsigned OOB = 0x80000000u;
constexpr unsigned SENT = 0xFFFFFFFFu;
constexpr float LOG2E = 1.4426950408889634f;
constexpr int SPIN_CAP = 1 << 16;

struct Params {
  const u16 *x, *w1a, *w1b, *wqkv, *wo, *wp1, *dw, *wp2, *w2a, *w2b;
  u16* out;
  unsigned char* ws;
};

DEV unsigned pk_bf16(float a, float b) {
  f32x2 v = {a, b};
  return __builtin_bit_cast(unsigned, __builtin_convertvector(v, bf16x2));
}
DEV float bf_lo(unsigned u) { return __uint_as_float(u << 16); }
DEV float bf_hi(unsigned u) { return __uint_as_float(u & 0xffff0000u); }
DEV float sig2(float y) { return __builtin_amdgcn_rcpf(1.f + __builtin_amdgcn_exp2f(-y)); }
template <int N> DEV void wait_vm() { asm volatile("s_waitcnt vmcnt(%0)" ::"i"(N) : "memory"); }
DEV void bar() { asm volatile("s_waitcnt lgkmcnt(0)\n\ts_barrier" ::: "memory"); }
DEV u32x4 mkrs(const void* base, unsigned bytes) { return (u32x4){(unsigned)(uintptr_t)base, (unsigned)((uintptr_t)base >> 32), bytes, 0x00020000u}; }
template <int SLOT, int STEP> DEV void dma(unsigned voff, const u32x4& rs, unsigned ring) {
  unsigned t;
  asm volatile("s_add_u32 m0, %3, %4\n\ts_mov_b32 %0, %5\n\tbuffer_load_dwordx4 %1, %2, %0 offen lds" : "=&s"(t) : "v"(voff), "s"(rs), "s"(ring), "i"(SLOT * 1024), "i"(STEP) : "memory", "scc");
}
template <int S0, int S1, int LD> DEV void issue(const u32x4& rs, unsigned voff, unsigned ring) {
  if constexpr (S0 < S1) {
    dma<S0, S0 * 32 * LD>(voff, rs, ring);
    issue<S0 + 1, S1, LD>(rs, voff, ring);
  }
}
#define SB() __builtin_amdgcn_sched_barrier(0)
template <int OFS> DEV void ld128(u32x4& d, unsigned voff, const u32x4& rs) {
  asm volatile("buffer_load_dwordx4 %0, %1, %2, 0 offen offset:%3 sc1" : "=v"(d) : "v"(voff), "s"(rs), "i"(OFS) : "memory");
}
DEV void ld64(u32x2& d, unsigned voff, const u32x4& rs) {
  asm volatile("buffer_load_dwordx2 %0, %1, %2, 0 offen sc1" : "=v"(d) : "v"(voff), "s"(rs) : "memory");
}
template <int OFS> DEV void st128(const u32x4& d, unsigned voff, const u32x4& rs) {
  asm volatile("buffer_store_dwordx4 %0, %1, %2, 0 offen offset:%3 sc1\n\ts_nop 1" ::"v"(d), "v"(voff), "s"(rs), "i"(OFS) : "memory");
}
DEV void st64(const u32x2& d, unsigned voff, const u32x4& rs) {
  asm volatile("buffer_store_dwordx2 %0, %1, %2, 0 offen sc1" ::"v"(d), "v"(voff), "s"(rs) : "memory");
}
DEV u32x4 clean(const u32x4& v) {
  return (u32x4){min(v.x, SENT - 1), v.y, min(v.z, SENT - 1), v.w};
}
DEV unsigned chk(unsigned m, const u32x4& v) { return max(max(m, v.x), v.z); }
DEV void keep(const u32x4& a) { asm volatile("" ::"v"(a)); }
DEV u32x4 trfrag(unsigned addr) {
  s16x4 lo = __builtin_amdgcn_ds_read_tr16_b64_v4i16(LDSP(s16x4, addr));
  s16x4 hi = __builtin_amdgcn_ds_read_tr16_b64_v4i16(LDSP(s16x4, addr + 512));
  u32x2 a = __builtin_bit_cast(u32x2, lo), b = __builtin_bit_cast(u32x2, hi);
  return (u32x4){a.x, a.y, b.x, b.y};
}
DEV f32x16 mfma(const u32x4& a, const u32x4& b, const f32x16& c) {
  return __builtin_amdgcn_mfma_f32_32x32x16_bf16(__builtin_bit_cast(bf16x8, a), __builtin_bit_cast(bf16x8, b), c, 0, 0, 0);
}
DEV u32x4 packhalf(const f32x16& v, int s) {
  return (u32x4){pk_bf16(v[8 * s], v[8 * s + 1]), pk_bf16(v[8 * s + 2], v[8 * s + 3]), pk_bf16(v[8 * s + 4], v[8 * s + 5]), pk_bf16(v[8 * s + 6], v[8 * s + 7])};
}
DEV float swap32_add(float a, float b) { auto r = __builtin_amdgcn_permlane32_swap(__float_as_uint(a), __float_as_uint(b), false, false); return __uint_as_float(r[0]) + __uint_as_float(r[1]); }
DEV float swap16_add(float a, float b) { auto r = __builtin_amdgcn_permlane16_swap(__float_as_uint(a), __float_as_uint(b), false, false); return __uint_as_float(r[0]) + __uint_as_float(r[1]); }
DEV float xhalf_max(float v) { auto r = __builtin_amdgcn_permlane32_swap(__float_as_uint(v), __float_as_uint(v), false, false); return fmaxf(__uint_as_float(r[0]), __uint_as_float(r[1])); }

DEV void ln_stats(const f32x16& v, unsigned stats, int w, int lane) {
  float s0 = v[0], s1 = v[1], q0 = v[0] * v[0], q1 = v[1] * v[1];
#pragma unroll
  for (int r = 2; r < 16; r += 2) { s0 += v[r]; s1 += v[r + 1]; q0 = fmaf(v[r], v[r], q0); q1 = fmaf(v[r + 1], v[r + 1], q1); }
  *LDSP(float, stats + w * 256 + (lane & 31) * 8 + (lane >> 5) * 4) = swap32_add(s0 + s1, q0 + q1);
}
DEV void ln_apply(const f32x16& v, unsigned stats, unsigned dst, int w, int lane, float scale) {
  f32x2 st[8];
#pragma unroll
  for (int p = 0; p < 8; ++p) st[p] = *LDSP(f32x2, stats + p * 256 + (lane & 31) * 8);
  f32x2 t = ((st[0] + st[1]) + (st[2] + st[3])) + ((st[4] + st[5]) + (st[6] + st[7]));
  float mean = t.x * (1.f / 256.f);
  float var = fmaxf(fmaf(-mean, mean, t.y * (1.f / 256.f)), 0.f);
  float rstd = __builtin_amdgcn_rsqf(var + 1e-5f) * scale;
  float nb = -mean * rstd;
  f32x16 o;
#pragma unroll
  for (int r = 0; r < 16; ++r) o[r] = fmaf(v[r], rstd, nb);
  *LDSP(u32x4, dst + (2 * w) * 1024 + lane * 16) = packhalf(o, 0);
  *LDSP(u32x4, dst + (2 * w + 1) * 1024 + lane * 16) = packhalf(o, 1);
}
DEV void put_act(const f32x16& v, unsigned buf, int w, int lane) {
  *LDSP(u32x4, buf + (2 * w) * 1024 + lane * 16) = packhalf(v, 0);
  *LDSP(u32x4, buf + (2 * w + 1) * 1024 + lane * 16) = packhalf(v, 1);
}
template <int S0, int S1> DEV void preread(u32x4 (&a)[16], unsigned rt) {
#pragma unroll
  for (int s = S0; s < S1; ++s) a[s] = trfrag(rt + s * 1024);
  SB();
}
template <int S0, int S1> DEV void pin(u32x4 (&a)[16]) {
#pragma unroll
  for (int s = S0; s < S1; ++s) asm volatile("" : "+v"(a[s]));
}
template <bool TRB> DEV void read_b(u32x4 (&b)[16], unsigned bb) {
#pragma unroll
  for (int s = 0; s < 16; ++s) b[s] = TRB ? trfrag(bb + (s << 10)) : *LDSP(u32x4, bb + (s << 10));
  SB();
}
template <int S0, int S1, bool SWAP> DEV void mm(f32x16& acc, const u32x4 (&a)[16], const u32x4 (&b)[16]) {
#pragma unroll
  for (int s = S0; s < S1; ++s) acc = SWAP ? mfma(b[s], a[s], acc) : mfma(a[s], b[s], acc);
  SB();
}
DEV void poll(unsigned voff, const u32x2& tag, const u32x4& rs) {
#pragma unroll 1
  for (int spins = 0; spins < SPIN_CAP; ++spins) {
    u32x2 v; ld64(v, voff, rs);
    asm volatile("s_waitcnt vmcnt(0)" : "+v"(v)::"memory");
    if (__all(v.x == tag.x && v.y == tag.y)) break;
    __builtin_amdgcn_s_sleep(1);
  }
}
template <int R0, int R1> DEV void silu2(f32x16& v) {
#pragma unroll
  for (int r = R0; r < R1; ++r) v[r] *= sig2(v[r]);
  asm volatile("" : "+v"(v));
}
#define STAMP(i)

__global__ void __launch_bounds__(512, 2) conformer_kernel(Params p) {
  extern __shared__ __attribute__((aligned(16))) char smem[];
  STAMP(0);
  const int wg = blockIdx.x;
  const int j = wg & 3, tile = ((wg >> 2) & 1) * 32 + (wg >> 3), cu = wg >> 3;
  const int b = tile >> 5, ti = tile & 31;
  const int w = __builtin_amdgcn_readfirstlane(threadIdx.x >> 6);
  const int nvalid = ti == 31 ? 8 : 32;
  const unsigned lbase = (unsigned)(size_t)(__attribute__((address_space(3))) char*)smem;
  const unsigned ring = lbase + w * 16384, bufA = lbase + BUFA, bufB = lbase + BUFB;
  const u32x4 rws = mkrs(p.ws, WS_USED);
  const int wgid = tile * 4 + j;
  int lane, h, tok;
  unsigned trofs, rt, drow, dcol, v1024, v256, l16, oP1, oKV, oP5st, oGLUst;
  auto lanevals = [&]() {
    asm volatile("v_mbcnt_lo_u32_b32 %0, -1, 0\n\tv_mbcnt_hi_u32_b32 %0, -1, %0" : "=v"(lane));
    h = lane >> 5; tok = lane & 31;
    trofs = (h * 4 + ((lane & 15) >> 2)) * 64 + ((lane >> 4) & 1) * 32 + (lane & 3) * 8;
    rt = ring + trofs;
    drow = lane >> 2; dcol = (lane & 3) * 8 + 32 * w;
    v1024 = (drow * 1024 + 256 * j + dcol) * 2; v256 = (drow * 256 + dcol) * 2;
    l16 = lane * 16;
    oP1 = OFF_P1 + (unsigned)(tile * 65536 + w * 2048) + l16;
    oKV = OFF_KV + (unsigned)((b * 4 + j) * 32 * 8192) + l16;
    oP5st = ((tok >> 3) == j) ? OOB : OFF_P5 + (unsigned)(((tile * 4 + (tok >> 3)) * 8 + w) * 2048 + j * 256 + h * 128 + (tok & 7) * 16);
    oGLUst = OFF_GLU + (unsigned)(tile * 16384 + (64 * j + 16 * w + (lane & 15)) * 64 + 32 * ((lane >> 4) & 1) + 16 * h);
  };
  lanevals();
  const u32x4 sent = {SENT, SENT, SENT, SENT};
  auto flagoff = [&](int hop, int t, int jj, int wave) { return OFF_FLAG + (unsigned)((((hop * 64 + t) * 4 + jj) * 8 + wave) * 128); };

  f32x16 xres;
  u32x2 tag;
  u32x4 wr[16], bf[16];
  const u32x4 r1a = mkrs(p.w1a, 524288), r1b = mkrs(p.w1b + 65536 * j, 131072);
  {
    unsigned junk, junk2;
    {
      const int row = 8 * cu + (lane >> 3), c = lane & 7;
      const u16* tb = w == 0 ? p.w1a : w == 1 ? p.w1b : w == 2 ? p.wqkv : w == 3 ? p.wo : w == 4 ? p.wp1 : w == 5 ? p.wp2 : w == 6 ? p.w2a : p.w2b;
      unsigned to;
      if (w == 0 || w == 6) to = row * 2048 + j * 512 + c * 64;
      else if (w == 1 || w == 7) to = (256 * j + row) * 512 + c * 64;
      else if (w == 2) to = row * 1536 + (min(c, 5) >> 1) * 512 + j * 128 + (c & 1) * 64;
      else if (w == 4) to = row * 1024 + ((c >> 1) & 1) * 512 + j * 128 + (c & 1) * 64;
      else to = row * 512 + c * 64;
      asm volatile("global_load_dword %0, %1, %2" : "=v"(junk) : "v"(to), "s"(tb) : "memory");
      asm volatile("global_load_dword %0, %1, %2" : "=v"(junk2) : "v"((unsigned)(min(cu, 30) * 512 + c * 64)), "s"(p.dw) : "memory");
    }
    const u32x4 rx = mkrs(p.x, 2000u * 512u);
    const unsigned xo = tok < nvalid ? (unsigned)((b * 1000 + ti * 32 + tok) * 512 + (32 * w + 16 * h) * 2) : OOB;
    u32x4 x0, x1;
    ld128<0>(x0, xo, rx); ld128<16>(x1, xo, rx);
    ld64(tag, OFF_EPOCH + b * 256, rws);
    issue<0, 8, 1024>(r1a, v1024, ring);
    asm volatile("s_waitcnt vmcnt(8)" : "+v"(x0), "+v"(x1), "+v"(tag), "+v"(junk), "+v"(junk2)::"memory");
    u64 e = (((u64)tag.y << 32) | tag.x) + 1;
    tag = (u32x2){(unsigned)e, (unsigned)(e >> 32)};
    auto a0 = __builtin_amdgcn_permlane32_swap(x0.x, x0.z, false, false), a1 = __builtin_amdgcn_permlane32_swap(x0.y, x0.w, false, false);
    auto c0 = __builtin_amdgcn_permlane32_swap(x1.x, x1.z, false, false), c1 = __builtin_amdgcn_permlane32_swap(x1.y, x1.w, false, false);
    const unsigned xv[8] = {a0[0], a1[0], c0[0], c1[0], a0[1], a1[1], c0[1], c1[1]};
#pragma unroll
    for (int r = 0; r < 8; ++r) { xres[2 * r] = bf_lo(xv[r]); xres[2 * r + 1] = bf_hi(xv[r]); }
  }
  STAMP(1);
  ln_stats(xres, bufB, w, lane);
  issue<8, 12, 1024>(r1a, v1024, ring);
  bar();
  ln_apply(xres, bufB, bufA, w, lane, LOG2E);
  issue<12, 16, 1024>(r1a, v1024, ring);
  wait_vm<8>(); preread<0, 8>(wr, rt);
  bar();
  STAMP(2);
  {
    f32x16 acc = {0};
    read_b<false>(bf, bufA + l16);
    pin<0, 8>(wr); issue<0, 2, 256>(r1b, v256, ring);
    mm<0, 4, false>(acc, wr, bf); issue<2, 4, 256>(r1b, v256, ring);
    wait_vm<8>(); preread<8, 12>(wr, rt);
    mm<4, 8, false>(acc, wr, bf); issue<4, 6, 256>(r1b, v256, ring);
    wait_vm<6>(); preread<12, 16>(wr, rt);
    mm<8, 12, false>(acc, wr, bf); issue<6, 8, 256>(r1b, v256, ring);
    mm<12, 16, false>(acc, wr, bf); issue<8, 10, 256>(r1b, v256, ring);
    silu2<0, 4>(acc); issue<10, 12, 256>(r1b, v256, ring);
    silu2<4, 8>(acc); issue<12, 14, 256>(r1b, v256, ring);
    silu2<8, 12>(acc); issue<14, 16, 256>(r1b, v256, ring);
    silu2<12, 16>(acc);
    put_act(acc, bufB, w, lane);
    wait_vm<8>(); preread<0, 8>(wr, rt);
  }
  bar();
  STAMP(3);
  const u32x4 rq = mkrs(p.wqkv, 393216), ro = mkrs(p.wo, 131072);
  {
    f32x16 acc = {0};
    read_b<false>(bf, bufB + l16);
    if (w < 4) { st128<0>(sent, OFF_ATT + (unsigned)(wgid * 4096 + w * 1024) + l16, rws); st128<0>(sent, oGLUst, rws); }
    mm<0, 4, false>(acc, wr, bf);
    wait_vm<4>(); preread<8, 12>(wr, rt);
    mm<4, 8, false>(acc, wr, bf);
    if (w < 4) wait_vm<2>(); else wait_vm<0>();
    preread<12, 16>(wr, rt);
    mm<8, 16, false>(acc, wr, bf);
#pragma unroll
    for (int r = 0; r < 16; ++r) acc[r] *= 0.5f / LOG2E;
    st128<0>(packhalf(acc, 0), oP1 + j * 16384, rws);
    st128<1024>(packhalf(acc, 1), oP1 + j * 16384, rws);
    STAMP(4);
    const unsigned v768 = (drow * 768 + (w < 4 ? 256 + (w >> 1) * 256 : 0) + 64 * j + (w & 1) * 32 + (lane & 3) * 8) * 2;
#pragma unroll
    for (int r = 0; r < 16; ++r) xres[r] += acc[r];
    asm volatile("" : "+v"(xres));
    wait_vm<0>();
    st64(tag, lane == 0 ? flagoff(0, tile, j, w) : OOB, rws);
    STAMP(5);
    if (w < 6) issue<0, 8, 768>(rq, v768, ring); else __builtin_amdgcn_s_sleep(8);
    poll(flagoff(0, tile, (j + 1 + (lane & 1) + ((lane >> 1) & 1)) & 3, w), tag, rws);
    STAMP(6);
    u32x4 pv[6];
    const unsigned o0 = oP1 + ((j + 1) & 3) * 16384, o1 = oP1 + ((j + 2) & 3) * 16384, o2 = oP1 + ((j + 3) & 3) * 16384;
    ld128<0>(pv[0], o0, rws); ld128<1024>(pv[1], o0, rws); ld128<0>(pv[2], o1, rws); ld128<1024>(pv[3], o1, rws); ld128<0>(pv[4], o2, rws); ld128<1024>(pv[5], o2, rws);
    if (w < 6) { issue<8, 16, 768>(rq, v768, ring); wait_vm<8>(); } else wait_vm<0>();
    asm volatile("" : "+v"(pv[0]), "+v"(pv[1]), "+v"(pv[2]), "+v"(pv[3]), "+v"(pv[4]), "+v"(pv[5])::"memory");
    STAMP(7);
#pragma unroll
    for (int q = 0; q < 3; ++q)
#pragma unroll
      for (int r = 0; r < 8; ++r) { xres[2 * r] += bf_lo(pv[2 * q + (r >> 2)][r & 3]); xres[2 * r + 1] += bf_hi(pv[2 * q + (r >> 2)][r & 3]); }
  }
  ln_stats(xres, bufA, w, lane);
  wait_vm<0>();
  if (w < 6) preread<0, 16>(wr, rt);
  bar();
  pin<0, 16>(wr);
  if (w >= 4) issue<0, 4, 256>(ro, v256, ring);
  ln_apply(xres, bufA, bufB, w, lane, 1.f);
  bar();
  STAMP(8);
  if (w < 4) {
    f32x16 acc = {0};
    read_b<false>(bf, bufB + l16);
    if (w >= 2) mm<0, 16, true>(acc, wr, bf); else mm<0, 16, false>(acc, wr, bf);
    st128<0>(packhalf(acc, 0), oKV + ti * 8192 + w * 2048, rws);
    st128<1024>(packhalf(acc, 1), oKV + ti * 8192 + w * 2048, rws);
    issue<0, 6, 256>(ro, v256, ring);
    wait_vm<6>();
    st64(tag, lane == 0 ? flagoff(1, tile, j, w) : OOB, rws);
    issue<6, 15, 256>(ro, v256, ring);
  } else {
    issue<4, 15, 256>(ro, v256, ring);
    if (w < 6) {
      f32x16 acc = {0};
      read_b<false>(bf, bufB + l16);
      mm<0, 16, false>(acc, wr, bf);
#pragma unroll
      for (int r = 0; r < 16; ++r) acc[r] *= 0.125f * LOG2E;
      put_act(acc, bufA, w - 4, lane);
    }
  }
  STAMP(9);
  bar();
  u32x4 qf[4];
#pragma unroll
  for (int s = 0; s < 4; ++s) qf[s] = *LDSP(u32x4, bufA + s * 1024 + l16);
  asm volatile("" : "+v"(qf[0]), "+v"(qf[1]), "+v"(qf[2]), "+v"(qf[3]));
  bar();
  poll(flagoff(1, b * 32 + 4 * w + ((lane >> 2) & 3), j, lane & 3), tag, rws);
  STAMP(10);
  {
    const __amdgpu_buffer_rsrc_t rkv = __builtin_amdgcn_make_buffer_rsrc(p.ws, 0, WS_USED, 0x00020000);
    u32x4 kf[4][4], vf[4][4];
    const unsigned o = oKV + (unsigned)(4 * w * 8192);
#pragma unroll
    for (int t = 0; t < 4; ++t)
#pragma unroll
      for (int s = 0; s < 4; ++s) kf[t][s] = __builtin_amdgcn_raw_buffer_load_b128(rkv, o + s * 1024, t * 8192, 16);
    SB();
    f32x16 sc[4];
    float mx = -INFINITY;
#pragma unroll
    for (int t = 0; t < 4; ++t) {
      f32x16 a = {0};
#pragma unroll
      for (int s = 0; s < 4; ++s) a = mfma(kf[t][s], qf[s], a);
      SB();
#pragma unroll
      for (int s = 0; s < 4; ++s) vf[t][s] = __builtin_amdgcn_raw_buffer_load_b128(rkv, o + s * 1024, t * 8192 + 4096, 16);
      SB();
      if (t == 3 && w == 7) {
#pragma unroll
        for (int r = 4; r < 16; ++r) a[r] = -INFINITY;
      }
      sc[t] = a;
      if (t > 0) {
#pragma unroll
        for (int r = 0; r < 16; r += 2) mx = fmaxf(fmaxf(mx, sc[t - 1][r]), sc[t - 1][r + 1]);
        SB();
      }
    }
#pragma unroll
    for (int r = 0; r < 16; r += 2) mx = fmaxf(fmaxf(mx, sc[3][r]), sc[3][r + 1]);
    mx = xhalf_max(mx);
    float s0 = 0.f, s1 = 0.f;
    f32x16 o0 = {0}, o1 = {0};
#pragma unroll
    for (int t = 0; t < 4; ++t) {
#pragma unroll
      for (int r = 0; r < 16; r += 2) {
        sc[t][r] = __builtin_amdgcn_exp2f(sc[t][r] - mx); s0 += sc[t][r];
        sc[t][r + 1] = __builtin_amdgcn_exp2f(sc[t][r + 1] - mx); s1 += sc[t][r + 1];
      }
      u32x4 p0 = packhalf(sc[t], 0), p1 = packhalf(sc[t], 1);
      o0 = mfma(vf[t][0], p0, o0); o1 = mfma(vf[t][2], p0, o1);
      o0 = mfma(vf[t][1], p1, o0); o1 = mfma(vf[t][3], p1, o1);
      SB();
    }
    lanevals();
    st128<0>(sent, oP5st, rws); st128<1024>(sent, oP5st, rws);
    float sum = s0 + s1;
    sum = swap32_add(sum, sum);
    put_act(o0, bufA + w * 4096, 0, lane);
    put_act(o1, bufA + w * 4096, 1, lane);
    *LDSP(float, ring + 15 * 1024 + tok * 8 + h * 4) = h ? sum : mx;
  }
  preread<0, 15>(wr, rt);
  STAMP(11);
  bar();
  {
    const int q2 = lane >> 1;
    f32x2 L[8];
    u32x2 pv[8];
#pragma unroll
    for (int q = 0; q < 8; ++q) L[q] = *LDSP(f32x2, lbase + q * 16384 + 15 * 1024 + q2 * 8);
#pragma unroll
    for (int q = 0; q < 8; ++q) pv[q] = *LDSP(u32x2, bufA + q * 4096 + w * 512 + lane * 8);
    SB();
    float lm = fmaxf(fmaxf(fmaxf(L[0].x, L[1].x), fmaxf(L[2].x, L[3].x)), fmaxf(fmaxf(L[4].x, L[5].x), fmaxf(L[6].x, L[7].x)));
    float om = 0.f, a[4] = {0, 0, 0, 0};
#pragma unroll
    for (int q = 0; q < 8; ++q) {
      float f = __builtin_amdgcn_exp2f(L[q].x - lm);
      om = fmaf(f, L[q].y, om);
      a[0] = fmaf(f, bf_lo(pv[q].x), a[0]); a[1] = fmaf(f, bf_hi(pv[q].x), a[1]);
      a[2] = fmaf(f, bf_lo(pv[q].y), a[2]); a[3] = fmaf(f, bf_hi(pv[q].y), a[3]);
    }
    om = __builtin_amdgcn_rcpf(om);
    st64((u32x2){min(pk_bf16(a[0] * om, a[1] * om), SENT - 1), pk_bf16(a[2] * om, a[3] * om)}, OFF_ATT + (unsigned)(wgid * 4096 + w * 512 + lane * 8), rws);
  }
  bar();
  STAMP(12);
  issue<15, 16, 256>(ro, v256, ring);
  if (ti == 0 && j == 0 && w == 4) st64(tag, lane == 0 ? OFF_EPOCH + b * 256 : OOB, rws);
  const u32x4 rp1 = mkrs(p.wp1, 262144), rp2 = mkrs(p.wp2, 131072), rdw = mkrs(p.dw, 31 * 512);
  const u32x4 r2a = mkrs(p.w2a, 524288), r2b = mkrs(p.w2b + 65536 * j, 131072);
  const unsigned v512 = (drow * 512 + 64 * j + 16 * w + (lane & 1) * 8 + ((lane >> 1) & 1) * 256) * 2;
  pin<0, 15>(wr);
  if (w < 4) issue<0, 5, 512>(rp1, v512, ring); else issue<0, 5, 256>(rp2, v256, ring);
  {
    const unsigned o = OFF_ATT + (unsigned)(tile * 16384 + w * 2048) + l16;
    u32x4 a0, a1;
    ld128<0>(a0, o, rws); ld128<1024>(a1, o, rws);
    if (w < 4) issue<5, 10, 512>(rp1, v512, ring); else issue<5, 10, 256>(rp2, v256, ring);
    asm volatile("s_waitcnt vmcnt(5)" : "+v"(a0), "+v"(a1)::"memory");
    preread<15, 16>(wr, rt);
#pragma unroll 1
    for (int spins = 0; spins < SPIN_CAP && !__all(chk(chk(0, a0), a1) != SENT); ++spins) {
      __builtin_amdgcn_s_sleep(1);
      ld128<0>(a0, o, rws); ld128<1024>(a1, o, rws);
      asm volatile("s_waitcnt vmcnt(0)" : "+v"(a0), "+v"(a1)::"memory");
    }
    *LDSP(u32x4, bufA + w * 2048 + l16) = a0;
    *LDSP(u32x4, bufA + w * 2048 + 1024 + l16) = a1;
  }
  bar();
  STAMP(13);
  {
    f32x16 acc = {0};
    read_b<false>(bf, bufA + l16);
    pin<15, 16>(wr);
    mm<0, 4, false>(acc, wr, bf);
    if (w < 4) issue<10, 12, 512>(rp1, v512, ring); else issue<10, 12, 256>(rp2, v256, ring);
    mm<4, 8, false>(acc, wr, bf);
    if (w < 4) issue<12, 14, 512>(rp1, v512, ring); else issue<12, 14, 256>(rp2, v256, ring);
    mm<8, 12, false>(acc, wr, bf);
    if (w < 4) issue<14, 16, 512>(rp1, v512, ring); else issue<14, 16, 256>(rp2, v256, ring);
    mm<12, 16, false>(acc, wr, bf);
#pragma unroll
    for (int r = 0; r < 16; ++r) xres[r] += acc[r];
  }
  ln_stats(xres, bufB, w, lane);
  wait_vm<0>();
  preread<0, 16>(wr, rt);
  bar();
  pin<0, 16>(wr);
  dma<14, 0>(v256, rdw, ring);
  dma<15, 8192>(v256, rdw, ring);
  ln_apply(xres, bufB, bufA, w, lane, 1.f);
  bar();
  STAMP(14);
  if (w < 4) {
    f32x16 acc = {0};
    read_b<false>(bf, bufA + l16);
    mm<0, 4, true>(acc, wr, bf); issue<0, 2, 256>(rp2, v256, ring);
    mm<4, 8, true>(acc, wr, bf); issue<2, 4, 256>(rp2, v256, ring);
    mm<8, 12, true>(acc, wr, bf); issue<4, 6, 256>(rp2, v256, ring);
    mm<12, 16, true>(acc, wr, bf); issue<6, 8, 256>(rp2, v256, ring);
    float g[8];
    const int e = (lane >> 4) & 1;
#pragma unroll
    for (int r = 0; r < 8; ++r) {
      auto sw = __builtin_amdgcn_permlane16_swap(__float_as_uint(acc[r]), __float_as_uint(acc[r + 8]), false, false);
      float v = __uint_as_float(sw[0]) * sig2(LOG2E * __uint_as_float(sw[1]));
      g[r] = ((r & 3) + 8 * (r >> 2) + 16 * e + 4 * h < nvalid) ? v : 0.f;
    }
    auto sx = __builtin_amdgcn_permlane32_swap(pk_bf16(g[0], g[1]), pk_bf16(g[4], g[5]), false, false);
    auto sy = __builtin_amdgcn_permlane32_swap(pk_bf16(g[2], g[3]), pk_bf16(g[6], g[7]), false, false);
    st128<0>(clean((u32x4){sx[0], sy[0], sx[1], sy[1]}), oGLUst, rws);
    wait_vm<9>();
  } else {
    issue<0, 14, 1024>(r2a, v1024, ring);
    wait_vm<14>();
  }
  STAMP(15);
  u32x2 tw[2][9];
  {
    const int c = lane >> 2, i = lane & 3;
    const unsigned tb = ring + 14 * 1024 + c * 2 - (i + 1) * 64;
#pragma unroll
    for (int g = 0; g < 2; ++g)
#pragma unroll
      for (int u = 0; u < 9; ++u) {
        unsigned e4[4];
#pragma unroll
        for (int k = 0; k < 4; ++k) {
          const int tap = 4 * u + k - 1 - i;
          const bool edge = 4 * u + k - 4 < 0 || 4 * u + k - 1 > 30;
          unsigned v = (4 * u + k == 0 || 4 * u + k == 35) ? 0u : *LDSP(u16, tb + (4 * u + k) * 64 + g * 32);
          e4[k] = (edge && (tap < 0 || tap > 30)) ? 0u : v;
        }
        tw[g][u] = (u32x2){e4[0] | (e4[1] << 16), e4[2] | (e4[3] << 16)};
      }
#pragma unroll
    for (int g = 0; g < 2; ++g)
#pragma unroll
      for (int u = 0; u < 9; ++u) asm volatile("" : "+v"(tw[g][u]));
  }
  if (w >= 4) issue<14, 16, 1024>(r2a, v1024, ring);
  bar();
  {
    const int c = lane >> 2, jj = lane & 3;
    unsigned o0 = OFF_GLU + (unsigned)((jj < 2 ? (tile - 1) * 16384 + (jj + 2) * 16 : tile * 16384 + (jj - 2) * 16) + (32 * w + c) * 64);
    unsigned o1 = OFF_GLU + (unsigned)((jj < 2 ? tile * 16384 + (jj + 2) * 16 : (tile + 1) * 16384 + (jj - 2) * 16) + (32 * w + c) * 64);
    if (jj < 2 && ti == 0) o0 = OOB;
    if (jj >= 2 && ti == 31) o1 = OOB;
    u32x4 c0[2], c1[2];
    ld128<0>(c0[0], o0, rws); ld128<0>(c1[0], o1, rws); ld128<1024>(c0[1], o0, rws); ld128<1024>(c1[1], o1, rws);
    if (w < 4) { issue<8, 16, 256>(rp2, v256, ring); wait_vm<8>(); } else wait_vm<0>();
    asm volatile("" : "+v"(c0[0]), "+v"(c1[0]), "+v"(c0[1]), "+v"(c1[1])::"memory");
#pragma unroll 1
    for (int spins = 0; spins < SPIN_CAP && !__all(chk(chk(chk(chk(0, c0[0]), c1[0]), c0[1]), c1[1]) != SENT); ++spins) {
      __builtin_amdgcn_s_sleep(1);
      ld128<0>(c0[0], o0, rws); ld128<0>(c1[0], o1, rws); ld128<1024>(c0[1], o0, rws); ld128<1024>(c1[1], o1, rws);
      asm volatile("s_waitcnt vmcnt(0)" : "+v"(c0[0]), "+v"(c1[0]), "+v"(c0[1]), "+v"(c1[1])::"memory");
    }
    STAMP(16);
    if (w < 4) preread<0, 8>(wr, rt);
    f32x4 d[2][2];
#pragma unroll
    for (int g = 0; g < 2; ++g) {
      u32x4 ch[5];
      ch[0] = c0[g]; ch[4] = c1[g];
#pragma unroll
      for (int dd = 0; dd < 4; ++dd) {
        ch[1][dd] = __builtin_amdgcn_mov_dpp(jj < 1 ? c1[g][dd] : c0[g][dd], 0x39, 0xf, 0xf, false);
        ch[2][dd] = __builtin_amdgcn_mov_dpp(jj < 2 ? c1[g][dd] : c0[g][dd], 0x4e, 0xf, 0xf, false);
        ch[3][dd] = __builtin_amdgcn_mov_dpp(jj < 3 ? c1[g][dd] : c0[g][dd], 0x93, 0xf, 0xf, false);
      }
      f32x4 d0 = {0, 0, 0, 0}, d1 = {0, 0, 0, 0};
#pragma unroll
      for (int u = 0; u < 9; ++u) {
        u32x2 q0 = {ch[u >> 1][2 * (u & 1)], ch[u >> 1][2 * (u & 1) + 1]};
        u32x2 q1 = {ch[(u + 1) >> 1][2 * ((u + 1) & 1)], ch[(u + 1) >> 1][2 * ((u + 1) & 1) + 1]};
        d0 = __builtin_amdgcn_mfma_f32_4x4x4bf16_1k(__builtin_bit_cast(s16x4, tw[g][u]), __builtin_bit_cast(s16x4, q0), d0, 0, 0, 0);
        d1 = __builtin_amdgcn_mfma_f32_4x4x4bf16_1k(__builtin_bit_cast(s16x4, tw[g][u]), __builtin_bit_cast(s16x4, q1), d1, 0, 0, 0);
      }
      d[g][0] = d0; d[g][1] = d1;
    }
    if (w < 4) { pin<0, 8>(wr); issue<0, 4, 1024>(r2a, v1024, ring); }
#pragma unroll
    for (int g = 0; g < 2; ++g) {
#pragma unroll
      for (int r = 0; r < 4; ++r) { d[g][0][r] *= sig2(LOG2E * d[g][0][r]); d[g][1][r] *= sig2(LOG2E * d[g][1][r]); }
      *LDSP(u32x4, bufB + (2 * w + g) * 1024 + l16) = (u32x4){pk_bf16(d[g][0][0], d[g][0][1]), pk_bf16(d[g][0][2], d[g][0][3]), pk_bf16(d[g][1][0], d[g][1][1]), pk_bf16(d[g][1][2], d[g][1][3])};
      if (w < 4) {
        if (g == 0) issue<4, 8, 1024>(r2a, v1024, ring);
        else { wait_vm<8>(); preread<8, 16>(wr, rt); }
      }
    }
  }
  bar();
  STAMP(17);
  {
    f32x16 acc = {0};
    read_b<true>(bf, bufB + trofs);
    pin<8, 16>(wr);
    if (w < 4) issue<8, 10, 1024>(r2a, v1024, ring);
    mm<0, 4, false>(acc, wr, bf);
    if (w < 4) issue<10, 13, 1024>(r2a, v1024, ring);
    mm<4, 8, false>(acc, wr, bf);
    if (w < 4) issue<13, 16, 1024>(r2a, v1024, ring);
    mm<8, 16, false>(acc, wr, bf);
#pragma unroll
    for (int r = 0; r < 16; ++r) xres[r] += acc[r];
  }
  ln_stats(xres, bufA, w, lane);
  wait_vm<0>();
  preread<0, 16>(wr, rt);
  bar();
  pin<0, 16>(wr);
  issue<0, 2, 256>(r2b, v256, ring);
  ln_apply(xres, bufA, bufB, w, lane, LOG2E);
  issue<2, 4, 256>(r2b, v256, ring);
  bar();
  STAMP(18);
  {
    f32x16 acc = {0};
    read_b<false>(bf, bufB + l16);
    mm<0, 4, false>(acc, wr, bf); issue<4, 6, 256>(r2b, v256, ring);
    mm<4, 8, false>(acc, wr, bf); issue<6, 8, 256>(r2b, v256, ring);
    mm<8, 12, false>(acc, wr, bf); issue<8, 10, 256>(r2b, v256, ring);
    mm<12, 16, false>(acc, wr, bf); issue<10, 12, 256>(r2b, v256, ring);
    silu2<0, 4>(acc); issue<12, 14, 256>(r2b, v256, ring);
    silu2<4, 8>(acc); issue<14, 16, 256>(r2b, v256, ring);
    silu2<8, 16>(acc);
    put_act(acc, bufA, w, lane);
    wait_vm<4>();
    preread<0, 12>(wr, rt);
  }
  bar();
  STAMP(19);
  {
    f32x16 acc = {0};
    read_b<false>(bf, bufA + l16);
    wait_vm<0>();
    preread<12, 16>(wr, rt);
    mm<0, 16, false>(acc, wr, bf);
#pragma unroll
    for (int r = 0; r < 16; ++r) acc[r] *= 0.5f / LOG2E;
    st128<0>(clean(packhalf(acc, 0)), oP5st, rws);
    st128<1024>(clean(packhalf(acc, 1)), oP5st, rws);
    STAMP(20);
    if ((tok >> 3) == j) {
#pragma unroll
      for (int g = 0; g < 4; ++g)
        *LDSP(f32x4, ring + ((g >> 1) + 2 * (g & 1)) * 256 + h * 128 + (tok & 7) * 16) =
            (f32x4){xres[4 * g] + acc[4 * g], xres[4 * g + 1] + acc[4 * g + 1], xres[4 * g + 2] + acc[4 * g + 2], xres[4 * g + 3] + acc[4 * g + 3]};
    }
    asm volatile("s_waitcnt lgkmcnt(0)" ::: "memory");
    f32x4 own = *LDSP(f32x4, ring + l16);
    asm volatile("" : "+v"(own));
    __builtin_amdgcn_s_sleep(8);
    const unsigned o = ((lane >> 4) == j) ? OOB : OFF_P5 + (unsigned)((wgid * 8 + w) * 2048) + l16;
    u32x4 a0, a1;
#pragma unroll 1
    for (int spins = 0; spins < SPIN_CAP; ++spins) {
      ld128<0>(a0, o, rws); ld128<1024>(a1, o, rws);
      asm volatile("s_waitcnt vmcnt(0)" : "+v"(a0), "+v"(a1)::"memory");
      if (__all(chk(chk(0, a0), a1) != SENT)) break;
      __builtin_amdgcn_s_sleep(1);
    }
    STAMP(21);
    float t8[8], v4[4];
#pragma unroll
    for (int r = 0; r < 2; ++r) {
      t8[2 * r] = swap32_add(bf_lo(a0[r]), bf_lo(a0[r + 2])); t8[2 * r + 1] = swap32_add(bf_hi(a0[r]), bf_hi(a0[r + 2]));
      t8[4 + 2 * r] = swap32_add(bf_lo(a1[r]), bf_lo(a1[r + 2])); t8[4 + 2 * r + 1] = swap32_add(bf_hi(a1[r]), bf_hi(a1[r + 2]));
    }
#pragma unroll
    for (int r = 0; r < 4; ++r) v4[r] = swap16_add(t8[r], t8[4 + r]) + own[r];
    float s = (v4[0] + v4[1]) + (v4[2] + v4[3]);
    float q = fmaf(v4[0], v4[0], v4[1] * v4[1]) + fmaf(v4[2], v4[2], v4[3] * v4[3]);
    float z = swap32_add(s, q);
    z = swap16_add(z, z);
    z += __uint_as_float(__builtin_amdgcn_mov_dpp(__float_as_uint(z), 0x128, 0xf, 0xf, false));
    if ((lane & 24) == 0) *LDSP(float, bufB + w * 64 + (lane & 7) * 8 + (lane >> 5) * 4) = z;
    bar();
    f32x2 st[8];
#pragma unroll
    for (int pp = 0; pp < 8; ++pp) st[pp] = *LDSP(f32x2, bufB + pp * 64 + (lane & 7) * 8);
    f32x2 t = ((st[0] + st[1]) + (st[2] + st[3])) + ((st[4] + st[5]) + (st[6] + st[7]));
    float mean = t.x * (1.f / 256.f);
    float var = fmaxf(fmaf(-mean, mean, t.y * (1.f / 256.f)), 0.f);
    float rstd = __builtin_amdgcn_rsqf(var + 1e-5f);
    const int otok = 8 * j + (lane & 7), qg = lane >> 4;
    if (otok < nvalid) {
      u16* dst = p.out + (size_t)(b * 1000 + ti * 32 + otok) * 256 + 32 * w + 8 * ((qg >> 1) + 2 * (qg & 1)) + 4 * ((lane >> 3) & 1);
      *(u32x2*)dst = (u32x2){pk_bf16((v4[0] - mean) * rstd, (v4[1] - mean) * rstd), pk_bf16((v4[2] - mean) * rstd, (v4[3] - mean) * rstd)};
    }
  }
}

extern "C" void kernel_launch(void* const* d_in, const int* in_sizes, int n_in, void* d_out, int out_size, void* d_ws, size_t ws_size,
                              hipStream_t stream) {
  Params p;
  p.x = (const u16*)d_in[0]; p.w1a = (const u16*)d_in[1]; p.w1b = (const u16*)d_in[2]; p.wqkv = (const u16*)d_in[3];
  p.wo = (const u16*)d_in[4]; p.wp1 = (const u16*)d_in[5]; p.dw = (const u16*)d_in[6]; p.wp2 = (const u16*)d_in[7];
  p.w2a = (const u16*)d_in[8]; p.w2b = (const u16*)d_in[9];
  p.out = (u16*)d_out; p.ws = (unsigned char*)d_ws;
  (void)hipFuncSetAttribute((const void*)conformer_kernel, hipFuncAttributeMaxDynamicSharedMemorySize, 163840);
  conformer_kernel<<<256, 512, 163840, stream>>>(p);
}
```

```cpp
#include <hip/hip_runtime.h>
#include <hip/hip_bf16.h>
#include <stdint.h>

typedef unsigned short u16;
typedef unsigned long long u64;
typedef float f32x16 __attribute__((ext_vector_type(16)));
typedef float f32x4 __attribute__((ext_vector_type(4)));
typedef float f32x2 __attribute__((ext_vector_type(2)));
typedef __bf16 bf16x8 __attribute__((ext_vector_type(8)));
typedef __bf16 bf16x2 __attribute__((ext_vector_type(2)));
typedef short s16x4 __attribute__((ext_vector_type(4)));
typedef unsigned u32x4 __attribute__((ext_vector_type(4)));
typedef unsigned u32x2 __attribute__((ext_vector_type(2)));
#define LDSP(T, off) ((__attribute__((address_space(3))) T*)(uintptr_t)(off))
#define GAS __attribute__((address_space(1)))
#define DEV __device__ __forceinline__

#define P5SLEEP 6
#define DMAFL ""
#define H1DMA 16
#define TLBT 0
#define APRIO 1

constexpr unsigned OFF_EPOCH = 0;
constexpr unsigned OFF_FLAG = 4096;
constexpr unsigned OFF_KV = 1u << 20;
constexpr int TS = 163840;
constexpr unsigned OFF_P1 = 3u << 20, OFF_ATT = OFF_P1 + 65536, OFF_GLU = OFF_ATT + 16384, OFF_P5 = OFF_GLU + 16384;
constexpr unsigned WS_USED = OFF_P1 + 64u * TS;
constexpr unsigned OFF_PROF = WS_USED;
constexpr unsigned BUFA = 0, BUFB = 16384, RING = 32768;
constexpr unsigned OOB = 0x80000000u;
constexpr unsigned SENT = 0xFFFFFFFFu;
constexpr float LOG2E = 1.4426950408889634f;
constexpr int CODE_LINES = 232;
constexpr int SPIN_CAP = 1 << 16;

struct Params {
  const u16 *x, *w1a, *w1b, *wqkv, *wo, *wp1, *dw, *wp2, *w2a, *w2b;
  u16* out;
  unsigned char* ws;
  struct { const u16* ptr; unsigned rs, jm, c2, cm, pad0, pad1; } tt[8];
};

DEV unsigned pk_bf16(float a, float b) {
  f32x2 v = {a, b};
  return __builtin_bit_cast(unsigned, __builtin_convertvector(v, bf16x2));
}
DEV float bf_lo(unsigned u) { return __uint_as_float(u << 16); }
DEV float bf_hi(unsigned u) { return __uint_as_float(u & 0xffff0000u); }
DEV float sig2(float y) { return __builtin_amdgcn_rcpf(1.f + __builtin_amdgcn_exp2f(-y)); }
template <int N> DEV void wait_vm() { asm volatile("s_waitcnt vmcnt(%0)" ::"i"(N) : "memory"); }
DEV void bar() { asm volatile("s_waitcnt lgkmcnt(0)\n\ts_barrier" ::: "memory"); }
DEV u32x4 mkrs(const void* base, unsigned bytes) { return (u32x4){(unsigned)(uintptr_t)base, (unsigned)((uintptr_t)base >> 32), bytes, 0x00020000u}; }
template <int SLOT, int STEP> DEV void dma(unsigned voff, const u32x4& rs, unsigned ring) {
  unsigned t;
  asm volatile("s_add_u32 m0, %3, %4\n\ts_mov_b32 %0, %5\n\tbuffer_load_dwordx4 %1, %2, %0 offen" DMAFL " lds" : "=&s"(t) : "v"(voff), "s"(rs), "s"(ring), "i"(SLOT * 1024), "i"(STEP) : "memory", "scc");
}
template <int S0, int S1, int LD> DEV void issue(const u32x4& rs, unsigned voff, unsigned ring) {
  if constexpr (S0 < S1) {
    dma<S0, S0 * 32 * LD>(voff, rs, ring);
    issue<S0 + 1, S1, LD>(rs, voff, ring);
  }
}
template <class T> DEV void PIN(T& x) { asm volatile("" : "+v"(x)); }
#define SB() __builtin_amdgcn_sched_barrier(0)
template <int OFS> DEV void ld128(u32x4& d, unsigned voff, const u32x4& rs) {
  asm volatile("buffer_load_dwordx4 %0, %1, %2, 0 offen offset:%3 sc1" : "=v"(d) : "v"(voff), "s"(rs), "i"(OFS) : "memory");
}
template <int OFS> DEV void ldx(u32x4& d, unsigned voff, const u32x4& rs) {
  asm volatile("buffer_load_dwordx4 %0, %1, %2, 0 offen offset:%3" : "=v"(d) : "v"(voff), "s"(rs), "i"(OFS) : "memory");
}
DEV void ld64(u32x2& d, unsigned voff, const u32x4& rs) {
  asm volatile("buffer_load_dwordx2 %0, %1, %2, 0 offen sc1" : "=v"(d) : "v"(voff), "s"(rs) : "memory");
}
template <int OFS> DEV void st128(const u32x4& d, unsigned voff, const u32x4& rs) {
  asm volatile("buffer_store_dwordx4 %0, %1, %2, 0 offen offset:%3 sc1\n\ts_nop 1" ::"v"(d), "v"(voff), "s"(rs), "i"(OFS) : "memory");
}
DEV void st64(const u32x2& d, unsigned voff, const u32x4& rs) {
  asm volatile("buffer_store_dwordx2 %0, %1, %2, 0 offen sc1" ::"v"(d), "v"(voff), "s"(rs) : "memory");
}
DEV u32x4 clean(const u32x4& v) {
  return (u32x4){min(v.x, SENT - 1), v.y, min(v.z, SENT - 1), v.w};
}
DEV unsigned chk(unsigned m, const u32x4& v) { return max(max(m, v.x), v.z); }
DEV void keep(const u32x4& a) { asm volatile("" ::"v"(a)); }
DEV u32x4 trfrag(unsigned addr, unsigned addr2) {
  s16x4 lo = __builtin_amdgcn_ds_read_tr16_b64_v4i16(LDSP(s16x4, addr));
  s16x4 hi = __builtin_amdgcn_ds_read_tr16_b64_v4i16(LDSP(s16x4, addr2));
  u32x2 a = __builtin_bit_cast(u32x2, lo), b = __builtin_bit_cast(u32x2, hi);
  return (u32x4){a.x, a.y, b.x, b.y};
}
DEV f32x16 mfma(const u32x4& a, const u32x4& b, const f32x16& c) {
  return __builtin_amdgcn_mfma_f32_32x32x16_bf16(__builtin_bit_cast(bf16x8, a), __builtin_bit_cast(bf16x8, b), c, 0, 0, 0);
}
DEV u32x4 packhalf(const f32x16& v, int s) {
  return (u32x4){pk_bf16(v[8 * s], v[8 * s + 1]), pk_bf16(v[8 * s + 2], v[8 * s + 3]), pk_bf16(v[8 * s + 4], v[8 * s + 5]), pk_bf16(v[8 * s + 6], v[8 * s + 7])};
}
DEV float swap32_add(float a, float b) { auto r = __builtin_amdgcn_permlane32_swap(__float_as_uint(a), __float_as_uint(b), false, false); return __uint_as_float(r[0]) + __uint_as_float(r[1]); }
DEV float swap16_add(float a, float b) { auto r = __builtin_amdgcn_permlane16_swap(__float_as_uint(a), __float_as_uint(b), false, false); return __uint_as_float(r[0]) + __uint_as_float(r[1]); }
DEV float xhalf_max(float v) { auto r = __builtin_amdgcn_permlane32_swap(__float_as_uint(v), __float_as_uint(v), false, false); return fmaxf(__uint_as_float(r[0]), __uint_as_float(r[1])); }

DEV void ln_stats(const f32x16& v, unsigned sw) {
  f32x2 s = {v[0], v[1]}, q = s * s;
#pragma unroll
  for (int r = 2; r < 16; r += 2) { f32x2 t = {v[r], v[r + 1]}; s += t; q = __builtin_elementwise_fma(t, t, q); }
  *LDSP(float, sw) = swap32_add(s.x + s.y, q.x + q.y);
}
template <int BUF> DEV void put_act(const f32x16& v, unsigned pa) {
  *LDSP(u32x4, pa + BUF) = packhalf(v, 0);
  *LDSP(u32x4, pa + BUF + 1024) = packhalf(v, 1);
}
template <int DST, int STR> DEV void ln_apply(const f32x16& v, unsigned sr, unsigned pa, float scale) {
  f32x2 st[8];
#pragma unroll
  for (int q = 0; q < 8; ++q) st[q] = *LDSP(f32x2, sr + q * STR);
  f32x2 t = ((st[0] + st[1]) + (st[2] + st[3])) + ((st[4] + st[5]) + (st[6] + st[7]));
  float mean = t.x * (1.f / 256.f);
  float var = fmaxf(fmaf(-mean, mean, t.y * (1.f / 256.f)), 0.f);
  float rstd = __builtin_amdgcn_rsqf(var + 1e-5f) * scale;
  float nb = -mean * rstd;
  f32x16 o;
#pragma unroll
  for (int r = 0; r < 16; ++r) o[r] = fmaf(v[r], rstd, nb);
  put_act<DST>(o, pa);
}
template <int S0, int S1> DEV void preread(u32x4 (&a)[16], unsigned rt) {
#pragma unroll
  for (int s = S0; s < S1; ++s) a[s] = trfrag(rt + s * 1024, rt + 16384 + s * 1024);
  SB();
}
template <int S0, int S1> DEV void preread(u32x4 (&a)[16], unsigned lo, unsigned hi) {
#pragma unroll
  for (int s = S0; s < S1; ++s) a[s] = trfrag(lo + s * 1024, hi + s * 1024);
  SB();
}
template <int S0, int S1> DEV void pin(u32x4 (&a)[16]) {
#pragma unroll
  for (int s = S0; s < S1; ++s) asm volatile("" : "+v"(a[s]));
}
template <int N> struct IC { static constexpr int v = N; };
template <int I, int N, class F> DEV void sfor(F&& f) { if constexpr (I < N) { f(IC<I>{}); sfor<I + 1, N>(f); } }
template <bool SWAP, bool TRB, class F> DEV f32x16 gemm(u32x4 (&a)[16], unsigned bb, f32x16 acc, F&& hook) {
  u32x4 b[16];
  constexpr int AH = TRB ? 3 : 4;
#pragma unroll
  for (int s = 0; s < AH; ++s) b[s] = TRB ? trfrag(bb + (s << 10), bb + (s << 10) + 512) : *LDSP(u32x4, bb + (s << 10));
  SB();
  sfor<0, 16>([&](auto I) {
    constexpr int s = I.v;
    hook(I);
    if constexpr (s + AH < 16) b[s + AH] = TRB ? trfrag(bb + ((s + AH) << 10), bb + ((s + AH) << 10) + 512) : *LDSP(u32x4, bb + ((s + AH) << 10));
    SB();
    acc = SWAP ? mfma(b[s], a[s], acc) : mfma(a[s], b[s], acc);
    SB();
  });
  return acc;
}
DEV void wait_lds() { asm volatile("s_waitcnt lgkmcnt(0)" ::: "memory"); }
DEV void bar_only() { asm volatile("s_barrier" ::: "memory"); }
DEV void poll(unsigned voff, const u32x2& tag, const u32x4& rs) {
#pragma unroll 1
  for (int spins = 0; spins < SPIN_CAP; ++spins) {
    u32x2 v; ld64(v, voff, rs);
    asm volatile("s_waitcnt vmcnt(0)" : "+v"(v)::"memory");
    if (__all(v.x == tag.x && v.y == tag.y)) break;
    __builtin_amdgcn_s_sleep(1);
  }
}
template <int R0, int R1> DEV void silu2(f32x16& v) {
#pragma unroll
  for (int r = R0; r < R1; ++r) v[r] *= sig2(v[r]);
  asm volatile("" : "+v"(v));
}
#define STAMP(i)
#define FS(i)

__global__ void __launch_bounds__(512, 2) conformer_kernel(Params p_) {
  STAMP(0);
  const int wg = blockIdx.x;
  const int j = wg & 3, tile = ((wg >> 2) & 1) * 32 + (wg >> 3), cu = wg >> 3;
  const int b = tile >> 5, ti = tile & 31;
  const int w = __builtin_amdgcn_readfirstlane(threadIdx.x >> 6);
  const __attribute__((address_space(4))) Params* kp = (const __attribute__((address_space(4))) Params*)__builtin_amdgcn_kernarg_segment_ptr();
  const u16* tptr = kp->tt[w].ptr;
  unsigned trs = kp->tt[w].rs, tjm = kp->tt[w].jm, tc2 = kp->tt[w].c2, tcm = kp->tt[w].cm;
  struct { const u16 *x, *w1a, *w1b, *wqkv, *wo, *wp1, *dw, *wp2, *w2a, *w2b; u16* out; unsigned char* ws; } p =
      {kp->x, kp->w1a, kp->w1b, kp->wqkv, kp->wo, kp->wp1, kp->dw, kp->wp2, kp->w2a, kp->w2b, kp->out, kp->ws};
  asm volatile("" : "+s"(tptr), "+s"(trs), "+s"(tjm), "+s"(tc2), "+s"(tcm), "+s"(p.x), "+s"(p.w1a), "+s"(p.w1b), "+s"(p.wqkv), "+s"(p.wo),
               "+s"(p.wp1), "+s"(p.dw), "+s"(p.wp2), "+s"(p.w2a), "+s"(p.w2b), "+s"(p.out), "+s"(p.ws));
  FS(13);
  const int nvalid = ti == 31 ? 8 : 32;
  const unsigned ring = RING + w * 16384;
  const u32x4 rws = mkrs(p.ws, WS_USED);
  int lane, h, tok;
  unsigned trofs, rt, crow, ccol, v1024, v256, l16, pa, sa, sw, sr, oGLUst;
  auto lane0 = [&]() {
    asm volatile("v_mbcnt_lo_u32_b32 %0, -1, 0\n\tv_mbcnt_hi_u32_b32 %0, -1, %0" : "=v"(lane));
    h = lane >> 5; tok = lane & 31;
  };
  auto lanevals = [&]() {
    trofs = (h * 4 + ((lane & 15) >> 2)) * 64 + ((lane >> 4) & 1) * 32 + (lane & 3) * 8;
    rt = RING + (w >> 1) * 32768 + (h * 4 + ((lane & 15) >> 2)) * 128 + (((w ^ (lane >> 3)) & 1) * 64) + ((lane >> 4) & 1) * 32 + (lane & 3) * 8;
    crow = (lane >> 3) + 8 * (w & 1);
    ccol = ((lane & 7) ^ ((lane >> 2) & 4)) * 16;
    v1024 = crow * 2048 + 512 * j + (w >> 1) * 128 + ccol; v256 = crow * 512 + (w >> 1) * 128 + ccol;
    l16 = lane * 16; pa = l16 + w * 2048; sr = tok * 8; sa = sr + h * 4; sw = sa + w * 256;
    oGLUst = OFF_GLU + (unsigned)(tile * TS + ((lane >> 4) & 1) * 8192 + (2 * j + (w >> 1)) * 1024 + (lane & 1) * 512 + ((w & 1) * 8 + ((lane & 15) >> 1)) * 32 + 16 * h);
    PIN(rt); PIN(v1024); PIN(v256); PIN(l16); PIN(pa); PIN(sw); PIN(sr); PIN(oGLUst);
  };
  lane0();
  const u32x4 sent = {SENT, SENT, SENT, SENT};
  auto flagoff = [&](int hop, int t, int jj, int wave) { return OFF_FLAG + (unsigned)((((hop * 64 + t) * 4 + jj) * 8 + wave) * 128); };
  const u32x4 r1a = mkrs(p.w1a, 524288), r1b = mkrs(p.w1b + 65536 * j, 131072);
  const f32x16 zero16 = {0};
  auto nohook = [&](auto) {};
  auto touch = [&]() { return (8 * cu + (lane >> 2)) * trs + j * tjm + min((unsigned)lane & 3u, tcm) * tc2; };
  f32x16 xres;
  u32x2 tag;
  u32x4 wr[16], wr2[16];
  unsigned oP1;
  {
    const u32x4 rx = mkrs(p.x, 2000u * 512u);
    const unsigned xo = tok < nvalid ? (unsigned)((b * 1000 + ti * 32 + tok) * 512 + (32 * w + 16 * h) * 2) : OOB;
    u32x4 x0, x1;
    ldx<0>(x0, xo, rx); ldx<16>(x1, xo, rx);
    unsigned junk = 0;
    if (lane < 32 && w < 4) asm volatile("global_load_dword %0, %1, %2" : "=v"(junk) : "v"(touch()), "s"(tptr) : "memory");
    if (w == 4 && lane < 8) {
      u64 pc;
      asm volatile("s_getpc_b64 %0" : "=s"(pc));
      asm volatile("global_load_dword %0, %1, %2" : "=v"(junk) : "v"(min(lane * 32 + cu, CODE_LINES - 1) * 128), "s"(pc) : "memory");
    }
    ld64(tag, OFF_EPOCH + b * 256, rws);
    SB();
    FS(14);
    lanevals();
    oP1 = OFF_P1 + (unsigned)(tile * TS) + pa;
    st128<0>(sent, oP1 + j * 16384, rws); st128<1024>(sent, oP1 + j * 16384, rws);
    wait_vm<3>();
    asm volatile("" : "+v"(x0), "+v"(x1), "+v"(junk)::"memory");
    FS(15);
    bar_only();
    issue<0, 4, 1024>(r1a, v1024, ring);
    SB();
    auto a0 = __builtin_amdgcn_permlane32_swap(x0.x, x0.z, false, false), a1 = __builtin_amdgcn_permlane32_swap(x0.y, x0.w, false, false);
    auto c0 = __builtin_amdgcn_permlane32_swap(x1.x, x1.z, false, false), c1 = __builtin_amdgcn_permlane32_swap(x1.y, x1.w, false, false);
    const unsigned xv[8] = {a0[0], a1[0], c0[0], c1[0], a0[1], a1[1], c0[1], c1[1]};
#pragma unroll
    for (int r = 0; r < 8; ++r) { xres[2 * r] = bf_lo(xv[r]); xres[2 * r + 1] = bf_hi(xv[r]); }
    STAMP(1);
    ln_stats(xres, BUFB + sw);
    issue<4, 8, 1024>(r1a, v1024, ring);
    bar();
    ln_apply<BUFA, 256>(xres, BUFB + sr, pa, LOG2E);
    SB();
    issue<8, 16, 1024>(r1a, v1024, ring);
    asm volatile("s_waitcnt vmcnt(8) lgkmcnt(0)" : "+v"(tag)::"memory");
    bar_only();
    preread<0, 4>(wr, rt);
    u64 e = (((u64)tag.y << 32) | tag.x) + 1;
    tag = (u32x2){(unsigned)e, (unsigned)(e >> 32)};
    if (w < 4) __builtin_amdgcn_s_setprio(2);
  }
  STAMP(2);
  {
    f32x16 acc = gemm<false, false>(wr, BUFA + l16, zero16, [&](auto I) {
      constexpr int s = I.v;
      if constexpr (s == 0) preread<4, 8>(wr, rt);
      if constexpr (s == 4) { pin<4, 8>(wr); wait_vm<0>(); bar_only(); if (w == 7) st64(tag, lane == 0 ? flagoff(0, tile, j, 0) : OOB, rws); issue<0, 2, 256>(r1b, v256, ring); preread<8, 12>(wr, rt); }
      if constexpr (s == 8) preread<12, 16>(wr, rt);
      if constexpr ((s & 1) && s > 4) issue<s / 2, s / 2 + 1, 256>(r1b, v256, ring);
      if constexpr (s == 15) { pin<15, 16>(wr); bar_only(); }
    });
    FS(10);
    sfor<0, 8>([&](auto I) { issue<8 + I.v, 9 + I.v, 256>(r1b, v256, ring); silu2<2 * I.v, 2 * I.v + 2>(acc); SB(); });
    FS(11);
    put_act<BUFB>(acc, pa);
    asm volatile("s_waitcnt vmcnt(8) lgkmcnt(0)" ::: "memory");
    FS(12);
    if (w < 4) { st128<0>(sent, OFF_ATT + (unsigned)(tile * TS + j * 4096 + w * 1024) + l16, rws); st128<0>(sent, oGLUst, rws); }
  }
  bar_only();
  preread<0, 4>(wr, rt);
  STAMP(3);
  const u32x4 rq = mkrs(p.wqkv, 393216), ro = mkrs(p.wo, 131072);
  {
    u32x2 fl;
    unsigned fo = flagoff(0, tile, (j + 1 + (lane & 1) + ((lane >> 1) & 1)) & 3, 0);
    f32x16 acc = gemm<false, false>(wr, BUFB + l16, zero16, [&](auto I) {
      constexpr int s = I.v;
      if constexpr (s == 0) preread<4, 8>(wr, rt);
      if constexpr (s == 5) { wait_vm<0>(); bar_only(); preread<8, 12>(wr, rt); }
      if constexpr (s == 8) preread<12, 16>(wr, rt);
      if constexpr (s == 6) ld64(fl, fo, rws);
    });
#pragma unroll
    for (int r = 0; r < 16; ++r) acc[r] *= 0.5f / LOG2E;
    st128<0>(clean(packhalf(acc, 0)), oP1 + j * 16384, rws);
    st128<1024>(clean(packhalf(acc, 1)), oP1 + j * 16384, rws);
    STAMP(4);
    unsigned v768 = crow * 1536 + (w < 4 ? 512 + (w >> 1) * 512 : 0) + 128 * j + ccol;
    PIN(v768);
    bar_only();
    if (w < 6) { issue<0, H1DMA, 768>(rq, v768, ring); wait_vm<H1DMA + 2>(); } else wait_vm<2>();
    PIN(fl);
    STAMP(5);
    xres += acc;
    PIN(xres);
    u32x4 idf[2];
    {
      const int n = ((tok >> 2) & 1) == h ? 2 * (tok >> 3) + ((tok >> 1) & 1) : 8;
      const unsigned one = 0x3F80u << (16 * (tok & 1));
#pragma unroll
      for (int q = 0; q < 8; ++q) idf[q >> 2][q & 3] = n == q ? one : 0u;
      PIN(idf[0]); PIN(idf[1]);
    }
    unsigned o0 = oP1 + ((j + 1) & 3) * 16384, o1 = oP1 + ((j + 2) & 3) * 16384, o2 = oP1 + ((j + 3) & 3) * 16384;
    PIN(o0); PIN(o1); PIN(o2);
    bar_only();
    if (!__all(fl.x == tag.x && fl.y == tag.y)) poll(fo, tag, rws);
    u32x4 pv[6];
#pragma unroll 1
    for (int spins = 0; spins < SPIN_CAP; ++spins) {
      ld128<0>(pv[0], o0, rws); ld128<1024>(pv[1], o0, rws); ld128<0>(pv[2], o1, rws); ld128<1024>(pv[3], o1, rws); ld128<0>(pv[4], o2, rws); ld128<1024>(pv[5], o2, rws);
      asm volatile("s_waitcnt vmcnt(0)" : "+v"(pv[0]), "+v"(pv[1]), "+v"(pv[2]), "+v"(pv[3]), "+v"(pv[4]), "+v"(pv[5])::"memory");
      if (__all(chk(chk(chk(chk(chk(chk(0, pv[0]), pv[1]), pv[2]), pv[3]), pv[4]), pv[5]) != SENT)) break;
    }
    STAMP(6);
#pragma unroll
    for (int q = 0; q < 6; ++q) xres = mfma(idf[q & 1], pv[q], xres);
    SB();
    STAMP(7);
  }
  ln_stats(xres, BUFA + sw);
  bar();
  ln_apply<BUFB, 256>(xres, BUFA + sr, pa, 1.f);
  wait_lds();
  if (w < 6) preread<0, 6>(wr, rt);
  bar_only();
  STAMP(8);
  unsigned oKV = OFF_KV + (unsigned)((b * 4 + j) * 32 * 8192) + l16;
  if (w < 4) {
    f32x16 acc;
    unsigned o = oKV + ti * 8192 + w * 2048;
    auto hk = [&](auto I) { if constexpr (I.v == 1) PIN(o); if constexpr (I.v < 10) preread<I.v + 6, I.v + 7>(wr, rt); };
    if (w >= 2) acc = gemm<true, false>(wr, BUFB + l16, zero16, hk); else acc = gemm<false, false>(wr, BUFB + l16, zero16, hk);
    st128<0>(packhalf(acc, 0), o, rws);
    st128<1024>(packhalf(acc, 1), o, rws);
    wait_vm<0>();
  } else if (w < 6) {
    f32x16 acc = gemm<false, false>(wr, BUFB + l16, zero16, [&](auto I) { if constexpr (I.v < 10) preread<I.v + 6, I.v + 7>(wr, rt); });
#pragma unroll
    for (int r = 0; r < 16; ++r) acc[r] *= 0.125f * LOG2E;
    put_act<BUFA>(acc, pa - 8192);
  } else {
    issue<0, 15, 256>(ro, v256, ring);
  }
  unsigned fo2 = lane == 0 ? flagoff(1, tile, j, 0) : OOB, to2 = OFF_KV + (unsigned)(((b * 4 + j) * 32 + ti) * 8192 + (w & 1) * 4096 + lane * 64);
  PIN(fo2); PIN(to2);
  STAMP(9);
  bar();
  unsigned junk3 = 0;
  if (w == 7) st64(tag, fo2, rws);
  if (w >= 6) asm volatile("buffer_load_dword %0, %1, %2, 0 offen sc1" : "=v"(junk3) : "v"(to2), "s"(rws) : "memory");
  unsigned junk4 = 0, junk5 = 0;
  if (lane < 32 && w >= 4) asm volatile("global_load_dword %0, %1, %2" : "=v"(junk4) : "v"(touch()), "s"(tptr) : "memory");
  if (lane < 4 && w == 5) asm volatile("global_load_dword %0, %1, %2" : "=v"(junk5) : "v"((unsigned)(min(cu, 30) * 512 + lane * 128)), "s"(p.dw) : "memory");
  u32x4 qf[4];
#pragma unroll
  for (int s = 0; s < 4; ++s) qf[s] = *LDSP(u32x4, BUFA + s * 1024 + l16);
  if (w < 6) issue<0, 15, 256>(ro, v256, ring); else __builtin_amdgcn_s_sleep(12);
  asm volatile("" : "+v"(qf[0]), "+v"(qf[1]), "+v"(qf[2]), "+v"(qf[3]));
  oKV += 4 * w * 8192;
  PIN(oKV);
  bar();
  poll(flagoff(1, b * 32 + 4 * w + (lane & 3), j, 0), tag, rws);
  PIN(junk3); PIN(junk4); PIN(junk5);
  STAMP(10);
  unsigned oP5st;
  {
    const __amdgpu_buffer_rsrc_t rkv = __builtin_amdgcn_make_buffer_rsrc(p.ws, 0, WS_USED, 0x00020000);
    u32x4 kf[4][4], vf[4][4];
#pragma unroll
    for (int t = 0; t < 4; ++t)
#pragma unroll
      for (int s = 0; s < 4; ++s) kf[t][s] = __builtin_amdgcn_raw_buffer_load_b128(rkv, oKV + s * 1024, t * 8192, 16);
    SB();
    f32x16 sc[4];
    float mx = -INFINITY;
#pragma unroll
    for (int t = 0; t < 4; ++t) {
      f32x16 a = {0};
#pragma unroll
      for (int s = 0; s < 4; ++s) a = mfma(kf[t][s], qf[s], a);
      SB();
#pragma unroll
      for (int s = 0; s < 4; ++s) vf[t][s] = __builtin_amdgcn_raw_buffer_load_b128(rkv, oKV + s * 1024, t * 8192 + 4096, 16);
      SB();
      if (t == 3 && w == 7) {
#pragma unroll
        for (int r = 4; r < 16; ++r) a[r] = -INFINITY;
      }
      sc[t] = a;
      if (t > 0) {
#pragma unroll
        for (int r = 0; r < 16; r += 2) mx = fmaxf(fmaxf(mx, sc[t - 1][r]), sc[t - 1][r + 1]);
        SB();
      }
    }
#pragma unroll
    for (int r = 0; r < 16; r += 2) mx = fmaxf(fmaxf(mx, sc[3][r]), sc[3][r + 1]);
    mx = xhalf_max(mx);
    const f32x2 mx2 = {mx, mx};
    f32x2 s2 = {0.f, 0.f};
    f32x16 o0 = {0}, o1 = {0};
#pragma unroll
    for (int t = 0; t < 4; ++t) {
      unsigned pk[8];
#pragma unroll
      for (int r = 0; r < 8; ++r) {
        f32x2 d = (f32x2){sc[t][2 * r], sc[t][2 * r + 1]} - mx2;
        d = (f32x2){__builtin_amdgcn_exp2f(d.x), __builtin_amdgcn_exp2f(d.y)};
        s2 += d;
        pk[r] = pk_bf16(d.x, d.y);
      }
      const u32x4 p0 = {pk[0], pk[1], pk[2], pk[3]}, p1 = {pk[4], pk[5], pk[6], pk[7]};
      o0 = mfma(vf[t][0], p0, o0); o1 = mfma(vf[t][2], p0, o1);
      o0 = mfma(vf[t][1], p1, o0); o1 = mfma(vf[t][3], p1, o1);
      SB();
    }
    lane0(); lanevals();
    float sum = s2.x + s2.y;
    sum = swap32_add(sum, sum);
    *LDSP(float, ring + 15 * 1024 + sa) = h ? sum : mx;
    SB();
    put_act<BUFA>(o0, pa + w * 2048);
    put_act<BUFA + 2048>(o1, pa + w * 2048);
  }
  STAMP(11);
  bar();
  {
    f32x2 L[8];
    u32x2 pv[8];
#pragma unroll
    for (int q = 0; q < 8; ++q) L[q] = *LDSP(f32x2, RING + q * 16384 + 15 * 1024 + (lane >> 1) * 8);
#pragma unroll
    for (int q = 0; q < 8; ++q) pv[q] = *LDSP(u32x2, BUFA + q * 4096 + w * 512 + lane * 8);
    SB();
    float lm = fmaxf(fmaxf(fmaxf(L[0].x, L[1].x), fmaxf(L[2].x, L[3].x)), fmaxf(fmaxf(L[4].x, L[5].x), fmaxf(L[6].x, L[7].x)));
    float om = 0.f;
    f32x2 a0 = {0, 0}, a1 = {0, 0};
#pragma unroll
    for (int q = 0; q < 8; ++q) {
      float f = __builtin_amdgcn_exp2f(L[q].x - lm);
      om = fmaf(f, L[q].y, om);
      const f32x2 f2 = {f, f};
      a0 = __builtin_elementwise_fma(f2, (f32x2){bf_lo(pv[q].x), bf_hi(pv[q].x)}, a0);
      a1 = __builtin_elementwise_fma(f2, (f32x2){bf_lo(pv[q].y), bf_hi(pv[q].y)}, a1);
    }
    om = __builtin_amdgcn_rcpf(om);
    a0 *= om; a1 *= om;
    st64((u32x2){min(pk_bf16(a0.x, a0.y), SENT - 1), pk_bf16(a1.x, a1.y)}, OFF_ATT + (unsigned)(tile * TS + j * 4096 + w * 512 + lane * 8), rws);
  }
  preread<0, 15>(wr, rt);
  oP5st = ((tok >> 3) == j) ? OOB : OFF_P5 + (unsigned)(tile * TS + ((tok >> 3) * 8 + w) * 2048 + j * 256 + h * 128 + (tok & 7) * 16);
  st128<0>(sent, oP5st, rws); st128<1024>(sent, oP5st, rws);
  bar();
  STAMP(12);
  issue<15, 16, 256>(ro, v256, ring);
  if (ti == 0 && j == 0 && w == 4) st64(tag, lane == 0 ? OFF_EPOCH + b * 256 : OOB, rws);
  const u32x4 rp1 = mkrs(p.wp1, 262144), rp2 = mkrs(p.wp2, 131072), rdw = mkrs(p.dw, 31 * 512);
  const u32x4 r2a = mkrs(p.w2a, 524288), r2b = mkrs(p.w2b + 65536 * j, 131072);
  const unsigned v512 = ((lane >> 2) * 512 + 64 * j + 16 * w + (lane & 1) * 8 + ((lane >> 1) & 1) * 256) * 2;
  pin<0, 15>(wr);
  { const unsigned vd = (lane >> 2) * 512 + (lane & 3) * 16 + 64 * w; dma<0, 0>(vd, rdw, BUFB + w * 2048); dma<1, 8192>(vd, rdw, BUFB + w * 2048); }
  if (w < 4) issue<0, 4, 512>(rp1, v512, ring); else issue<0, 4, 1024>(r2a, v1024, ring);
  unsigned z31 = BUFB + w * 2048 + 31 * 64 + (lane & 15) * 4, sw3 = ring + 3 * 1024 + sa, sr3 = RING + 3 * 1024 + sr;
  unsigned r1lo = w < 4 ? ring + trofs : rt, r1hi = r1lo + (w < 4 ? 512 : 16384);
  PIN(r1lo); PIN(r1hi); PIN(trofs); PIN(z31); PIN(sw3); PIN(sr3);
  {
    const unsigned o = OFF_ATT + (unsigned)(tile * TS) + pa;
    u32x4 a0, a1;
    ld128<0>(a0, o, rws); ld128<1024>(a1, o, rws);
    bar_only();
    if (w < 4) issue<4, 12, 512>(rp1, v512, ring); else issue<4, 12, 1024>(r2a, v1024, ring);
    asm volatile("s_waitcnt vmcnt(8)" : "+v"(a0), "+v"(a1)::"memory");
    *LDSP(unsigned, z31) = 0;
#pragma unroll 1
    for (int spins = 0; spins < SPIN_CAP && !__all(chk(chk(0, a0), a1) != SENT); ++spins) {
      ld128<0>(a0, o, rws); ld128<1024>(a1, o, rws);
      asm volatile("s_waitcnt vmcnt(0)" : "+v"(a0), "+v"(a1)::"memory");
    }
    *LDSP(u32x4, BUFA + pa) = a0;
    *LDSP(u32x4, BUFA + pa + 1024) = a1;
  }
  bar();
  STAMP(13);
  xres = gemm<false, false>(wr, BUFA + l16, xres, [&](auto I) {
    constexpr int s = I.v;
    if constexpr (s == 1) { preread<15, 16>(wr, rt); preread<0, 4>(wr2, r1lo, r1hi); }
    if constexpr (s == 1 || s == 3 || s == 5) { if (w < 4) issue<12 + s / 2, 13 + s / 2, 512>(rp1, v512, ring); else issue<12 + s / 2, 13 + s / 2, 1024>(r2a, v1024, ring); }
    if constexpr (s == 8) { pin<15, 16>(wr); pin<0, 4>(wr2); wait_vm<3>(); bar_only(); preread<4, 8>(wr2, r1lo, r1hi); }
    if constexpr (s == 9) { if (w < 4) issue<15, 16, 512>(rp1, v512, ring); else issue<15, 16, 1024>(r2a, v1024, ring); }
    if constexpr (s == 12) preread<8, 12>(wr2, r1lo, r1hi);
    if constexpr (s == 11 || s == 13 || s == 15) issue<s / 2 - 5, s / 2 - 4, 256>(rp2, v256, ring);
  });
  ln_stats(xres, sw3);
  wait_vm<3>();
  bar();
  pin<4, 12>(wr2);
  if (w < 4) issue<4, 8, 256>(rp2, v256, ring);
  ln_apply<BUFA, 16384>(xres, sr3, pa, 1.f);
  SB();
  preread<12, 16>(wr2, r1lo, r1hi);
  if (w < 4) issue<8, 12, 256>(rp2, v256, ring);
  bar();
  STAMP(14);
  pin<12, 16>(wr2);
  u32x2 tw[2][9];
  auto build_tw = [&]() {
  {
    const int c = lane >> 2, i = lane & 3;
    const unsigned tc = BUFB + w * 2048 + c * 4, tb = tc - (i + 1) * 64;
    unsigned t[36];
#pragma unroll
    for (int n = 0; n < 36; ++n) {
      if (n == 0 || n == 35) t[n] = 0;
      else if (n < 4 || n > 32) t[n] = *LDSP(unsigned, tc + min((unsigned)(n - 1 - i), 31u) * 64);
      else t[n] = *LDSP(unsigned, tb + n * 64);
    }
#pragma unroll
    for (int u = 0; u < 9; ++u) {
      tw[0][u] = (u32x2){__builtin_amdgcn_perm(t[4 * u + 1], t[4 * u], 0x05040100u), __builtin_amdgcn_perm(t[4 * u + 3], t[4 * u + 2], 0x05040100u)};
      tw[1][u] = (u32x2){__builtin_amdgcn_perm(t[4 * u + 1], t[4 * u], 0x07060302u), __builtin_amdgcn_perm(t[4 * u + 3], t[4 * u + 2], 0x07060302u)};
      PIN(tw[0][u]); PIN(tw[1][u]);
    }
  }
  };
  if (w < 4) {
    f32x16 acc = gemm<true, false>(wr2, BUFA + l16, zero16, [&](auto I) {
      if constexpr (I.v == 1) { issue<3, 4, 256>(rp2, v256, ring); issue<12, 16, 256>(rp2, v256, ring); }
    });
    float g[8];
    const int e = (lane >> 4) & 1;
#pragma unroll
    for (int r = 0; r < 8; ++r) {
      auto sw = __builtin_amdgcn_permlane16_swap(__float_as_uint(acc[r]), __float_as_uint(acc[r + 8]), false, false);
      g[r] = __uint_as_float(sw[0]) * sig2(LOG2E * __uint_as_float(sw[1]));
    }
    if (ti == 31) {
#pragma unroll
      for (int r = 0; r < 8; ++r) g[r] = ((r & 3) + 8 * (r >> 2) + 16 * e + 4 * h < 8) ? g[r] : 0.f;
    }
    auto sx = __builtin_amdgcn_permlane32_swap(pk_bf16(g[0], g[1]), pk_bf16(g[4], g[5]), false, false);
    auto sy = __builtin_amdgcn_permlane32_swap(pk_bf16(g[2], g[3]), pk_bf16(g[6], g[7]), false, false);
    st128<0>(clean((u32x4){sx[0], sy[0], sx[1], sy[1]}), oGLUst, rws);
    wait_vm<1>();
  } else {
    issue<3, 16, 256>(rp2, v256, ring);
    wait_vm<0>();
  }
  STAMP(15);
  bar_only();
  preread<0, 16>(wr, rt);
  bar();
  pin<0, 16>(wr);
  if (w < 4) issue<0, 6, 1024>(r2a, v1024, ring); else issue<0, 4, 256>(r2b, v256, ring);
  build_tw();
  {
    const int c = lane >> 2, jj = lane & 3;
    unsigned o0 = OFF_GLU + (unsigned)(tile * TS + (jj < 2 ? 8192 - TS : 0) + w * 1024 + c * 32 + (jj & 1) * 16), o1 = o0 + TS;
    if (jj < 2 && ti == 0) o0 = OOB;
    if (jj >= 2 && ti == 31) o1 = OOB;
    unsigned pc = BUFB + w * 2048 + c * 128 + jj * 16;
    PIN(o0); PIN(o1); PIN(pc);
    u32x4 c0[2], c1[2];
#pragma unroll 1
    for (int spins = 0; spins < SPIN_CAP; ++spins) {
      ld128<0>(c0[0], o0, rws); ld128<0>(c1[0], o1, rws); ld128<512>(c0[1], o0, rws); ld128<512>(c1[1], o1, rws);
      asm volatile("s_waitcnt vmcnt(0)" : "+v"(c0[0]), "+v"(c1[0]), "+v"(c0[1]), "+v"(c1[1])::"memory");
      if (__all(chk(chk(chk(chk(0, c0[0]), c1[0]), c0[1]), c1[1]) != SENT)) break;
    }
    STAMP(16);
    if (w < 4) issue<6, 11, 1024>(r2a, v1024, ring);
    f32x4 d[2][2];
#pragma unroll
    for (int g = 0; g < 2; ++g) {
      u32x4 ch[5];
      ch[0] = c0[g]; ch[4] = c1[g];
#pragma unroll
      for (int dd = 0; dd < 4; ++dd) {
        ch[1][dd] = __builtin_amdgcn_mov_dpp(jj < 1 ? c1[g][dd] : c0[g][dd], 0x39, 0xf, 0xf, false);
        ch[2][dd] = __builtin_amdgcn_mov_dpp(jj < 2 ? c1[g][dd] : c0[g][dd], 0x4e, 0xf, 0xf, false);
        ch[3][dd] = __builtin_amdgcn_mov_dpp(jj < 3 ? c1[g][dd] : c0[g][dd], 0x93, 0xf, 0xf, false);
      }
      f32x4 d0 = {0, 0, 0, 0}, d1 = {0, 0, 0, 0};
#pragma unroll
      for (int u = 0; u < 9; ++u) {
        u32x2 q0 = {ch[u >> 1][2 * (u & 1)], ch[u >> 1][2 * (u & 1) + 1]};
        u32x2 q1 = {ch[(u + 1) >> 1][2 * ((u + 1) & 1)], ch[(u + 1) >> 1][2 * ((u + 1) & 1) + 1]};
        d0 = __builtin_amdgcn_mfma_f32_4x4x4bf16_1k(__builtin_bit_cast(s16x4, tw[g][u]), __builtin_bit_cast(s16x4, q0), d0, 0, 0, 0);
        d1 = __builtin_amdgcn_mfma_f32_4x4x4bf16_1k(__builtin_bit_cast(s16x4, tw[g][u]), __builtin_bit_cast(s16x4, q1), d1, 0, 0, 0);
      }
      d[g][0] = d0; d[g][1] = d1;
      if (g == 0 && w < 4) issue<11, 16, 1024>(r2a, v1024, ring);
    }
#pragma unroll
    for (int g = 0; g < 2; ++g) {
#pragma unroll
      for (int r = 0; r < 4; ++r) { d[g][0][r] *= sig2(LOG2E * d[g][0][r]); d[g][1][r] *= sig2(LOG2E * d[g][1][r]); }
      *LDSP(u32x4, pc + g * 64) = (u32x4){pk_bf16(d[g][0][0], d[g][0][1]), pk_bf16(d[g][0][2], d[g][0][3]), pk_bf16(d[g][1][0], d[g][1][1]), pk_bf16(d[g][1][2], d[g][1][3])};
    }
    if (w < 4) wait_vm<0>(); else issue<4, 8, 256>(r2b, v256, ring);
  }
  bar();
  STAMP(17);
  xres = gemm<false, true>(wr, trofs + BUFB, xres, [&](auto I) {
    constexpr int s = I.v;
    if constexpr (s & 1) { if (w < 4) preread<s - 1, s + 1>(wr2, rt); else issue<8 + s / 2, 9 + s / 2, 256>(r2b, v256, ring); }
  });
  ln_stats(xres, BUFA + sw);
  bar();
  pin<0, 16>(wr2);
  if (w < 4) issue<0, 6, 256>(r2b, v256, ring);
  ln_apply<BUFB, 256>(xres, BUFA + sr, pa, LOG2E);
  SB();
  if (w < 4) issue<6, 12, 256>(r2b, v256, ring);
  bar();
  STAMP(18);
  {
    f32x16 acc = gemm<false, false>(wr2, BUFB + l16, zero16, [&](auto I) {
      constexpr int s = I.v;
      if constexpr (s == 1) { if (w < 4) issue<12, 16, 256>(r2b, v256, ring); }
    });
    silu2<0, 8>(acc);
    wait_vm<0>(); bar_only();
    preread<0, 8>(wr, rt);
    silu2<8, 16>(acc);
    put_act<BUFA>(acc, pa);
  }
  bar();
  STAMP(19);
  {
    unsigned o = ((lane >> 4) == j) ? OOB : OFF_P5 + (unsigned)(tile * TS + j * 16384) + pa;
    unsigned oa = ring + h * 128 + (tok & 7) * 16;
    f32x16 acc = gemm<false, false>(wr, BUFA + l16, zero16, [&](auto I) {
      if constexpr (I.v == 1) { PIN(o); PIN(oa); }
      if constexpr (I.v < 8) preread<I.v + 8, I.v + 9>(wr, rt);
    });
#pragma unroll
    for (int r = 0; r < 16; ++r) acc[r] *= 0.5f / LOG2E;
    st128<0>(clean(packhalf(acc, 0)), oP5st, rws);
    st128<1024>(clean(packhalf(acc, 1)), oP5st, rws);
    STAMP(20);
    xres += acc;
    if ((tok >> 3) == j) {
#pragma unroll
      for (int g = 0; g < 4; ++g)
        *LDSP(f32x4, oa + ((g >> 1) + 2 * (g & 1)) * 256) = (f32x4){xres[4 * g], xres[4 * g + 1], xres[4 * g + 2], xres[4 * g + 3]};
    }
    wait_lds();
    f32x4 own = *LDSP(f32x4, ring + l16);
    const int otok = 8 * j + (lane & 7), qg = lane >> 4;
    unsigned oo = otok < nvalid ? (unsigned)(((b * 1000 + ti * 32 + otok) * 256 + 32 * w + 8 * ((qg >> 1) + 2 * (qg & 1)) + 4 * ((lane >> 3) & 1)) * 2) : OOB;
    unsigned sf = BUFB + (lane & 7) * 8, sg = (lane & 24) == 0 ? sf + w * 64 + h * 4 : BUFB + 1024;
    const u32x4 rout = mkrs(p.out, 2000u * 512u);
    PIN(own); PIN(oo); PIN(sf); PIN(sg);
    __builtin_amdgcn_s_sleep(P5SLEEP);
    u32x4 a0, a1;
#pragma unroll 1
    for (int spins = 0; spins < SPIN_CAP; ++spins) {
      ld128<0>(a0, o, rws); ld128<1024>(a1, o, rws);
      asm volatile("s_waitcnt vmcnt(0)" : "+v"(a0), "+v"(a1)::"memory");
      if (__all(chk(chk(0, a0), a1) != SENT)) break;
    }
    STAMP(21);
    float t8[8], v4[4];
#pragma unroll
    for (int r = 0; r < 2; ++r) {
      auto s0 = __builtin_amdgcn_permlane32_swap(a0[r], a0[r + 2], false, false), s1 = __builtin_amdgcn_permlane32_swap(a1[r], a1[r + 2], false, false);
      t8[2 * r] = bf_lo(s0[0]) + bf_lo(s0[1]); t8[2 * r + 1] = bf_hi(s0[0]) + bf_hi(s0[1]);
      t8[4 + 2 * r] = bf_lo(s1[0]) + bf_lo(s1[1]); t8[4 + 2 * r + 1] = bf_hi(s1[0]) + bf_hi(s1[1]);
    }
#pragma unroll
    for (int r = 0; r < 4; ++r) v4[r] = swap16_add(t8[r], t8[4 + r]) + own[r];
    float s = (v4[0] + v4[1]) + (v4[2] + v4[3]);
    float q = fmaf(v4[0], v4[0], v4[1] * v4[1]) + fmaf(v4[2], v4[2], v4[3] * v4[3]);
    float z = swap32_add(s, q);
    z = swap16_add(z, z);
    z += __uint_as_float(__builtin_amdgcn_mov_dpp(__float_as_uint(z), 0x128, 0xf, 0xf, false));
    *LDSP(float, sg) = z;
    bar();
    f32x2 st[8];
#pragma unroll
    for (int pp = 0; pp < 8; ++pp) st[pp] = *LDSP(f32x2, sf + pp * 64);
    f32x2 t = ((st[0] + st[1]) + (st[2] + st[3])) + ((st[4] + st[5]) + (st[6] + st[7]));
    float mean = t.x * (1.f / 256.f);
    float var = fmaxf(fmaf(-mean, mean, t.y * (1.f / 256.f)), 0.f);
    float rstd = __builtin_amdgcn_rsqf(var + 1e-5f);
    const u32x2 ov = {pk_bf16((v4[0] - mean) * rstd, (v4[1] - mean) * rstd), pk_bf16((v4[2] - mean) * rstd, (v4[3] - mean) * rstd)};
    asm volatile("buffer_store_dwordx2 %0, %1, %2, 0 offen" ::"v"(ov), "v"(oo), "s"(rout) : "memory");
  }
}

extern "C" void kernel_launch(void* const* d_in, const int* in_sizes, int n_in, void* d_out, int out_size, void* d_ws, size_t ws_size,
                              hipStream_t stream) {
  Params p;
  p.x = (const u16*)d_in[0]; p.w1a = (const u16*)d_in[1]; p.w1b = (const u16*)d_in[2]; p.wqkv = (const u16*)d_in[3];
  p.wo = (const u16*)d_in[4]; p.wp1 = (const u16*)d_in[5]; p.dw = (const u16*)d_in[6]; p.wp2 = (const u16*)d_in[7];
  p.w2a = (const u16*)d_in[8]; p.w2b = (const u16*)d_in[9];
  p.out = (u16*)d_out; p.ws = (unsigned char*)d_ws;
  const u16* tp[8] = {p.w1a, p.w1b, p.wqkv, p.wo, p.wp1, p.wp2, p.w2a, p.w2b};
  const unsigned rs[8] = {2048, 512, 1536, 512, 1024, 512, 2048, 512}, jm[8] = {512, 131072, 128, 0, 128, 0, 512, 131072};
  const unsigned c2[8] = {128, 128, 512, 128, 512, 128, 128, 128}, cm[8] = {3, 3, 2, 3, 1, 3, 3, 3};
  for (int i = 0; i < 8; ++i) { p.tt[i].ptr = tp[i]; p.tt[i].rs = rs[i]; p.tt[i].jm = jm[i]; p.tt[i].c2 = c2[i]; p.tt[i].cm = cm[i]; p.tt[i].pad0 = 0; p.tt[i].pad1 = 0; }
  (void)hipFuncSetAttribute((const void*)conformer_kernel, hipFuncAttributeMaxDynamicSharedMemorySize, 163840);
  conformer_kernel<<<256, 512, 163840, stream>>>(p);
}
```

```cpp
#include <hip/hip_runtime.h>
#include <hip/hip_bf16.h>
#include <stdint.h>

typedef unsigned short u16;
typedef unsigned long long u64;
typedef float f32x16 __attribute__((ext_vector_type(16)));
typedef float f32x4 __attribute__((ext_vector_type(4)));
typedef float f32x2 __attribute__((ext_vector_type(2)));
typedef __bf16 bf16x8 __attribute__((ext_vector_type(8)));
typedef __bf16 bf16x2 __attribute__((ext_vector_type(2)));
typedef short s16x4 __attribute__((ext_vector_type(4)));
typedef unsigned u32x4 __attribute__((ext_vector_type(4)));
typedef unsigned u32x2 __attribute__((ext_vector_type(2)));
#define LDSP(T, off) ((__attribute__((address_space(3))) T*)(uintptr_t)(off))
#define GAS __attribute__((address_space(1)))
#define DEV __device__ __forceinline__

#define P5SLEEP 6
#define DMAFL ""
#define H1DMA 16
#define T1W 4
#define NOWAIT 0
#define CODET 0
#define H2SLEEP 12
#define APRIO 1

constexpr unsigned OFF_EPOCH = 0;
constexpr unsigned OFF_FLAG = 4096;
constexpr unsigned OFF_KV = 1u << 20;
constexpr int TS = 163840;
constexpr unsigned OFF_P1 = 3u << 20, OFF_ATT = OFF_P1 + 65536, OFF_GLU = OFF_ATT + 16384, OFF_P5 = OFF_GLU + 16384;
constexpr unsigned WS_USED = OFF_P1 + 64u * TS;
constexpr unsigned OFF_PROF = WS_USED;
constexpr unsigned BUFA = 0, BUFB = 16384, RING = 32768;
constexpr unsigned OOB = 0x80000000u;
constexpr unsigned SENT = 0xFFFFFFFFu;
constexpr float LOG2E = 1.4426950408889634f;
constexpr int CODE_LINES = 200;
constexpr int SPIN_CAP = 1 << 16;

struct Params {
  const u16 *x, *w1a, *w1b, *wqkv, *wo, *wp1, *dw, *wp2, *w2a, *w2b;
  u16* out;
  unsigned char* ws;
  struct { const u16* ptr; unsigned rs, jm, c2, cm, pad0, pad1; } tt[8];
};

DEV unsigned pk_bf16(float a, float b) {
  f32x2 v = {a, b};
  return __builtin_bit_cast(unsigned, __builtin_convertvector(v, bf16x2));
}
DEV float bf_lo(unsigned u) { return __uint_as_float(u << 16); }
DEV float bf_hi(unsigned u) { return __uint_as_float(u & 0xffff0000u); }
DEV float sig2(float y) { return __builtin_amdgcn_rcpf(1.f + __builtin_amdgcn_exp2f(-y)); }
template <int N> DEV void wait_vm() { asm volatile("s_waitcnt vmcnt(%0)" ::"i"(N) : "memory"); }
DEV void bar() { asm volatile("s_waitcnt lgkmcnt(0)\n\ts_barrier" ::: "memory"); }
DEV u32x4 mkrs(const void* base, unsigned bytes) { return (u32x4){(unsigned)(uintptr_t)base, (unsigned)((uintptr_t)base >> 32), bytes, 0x00020000u}; }
template <int SLOT, int STEP> DEV void dma(unsigned voff, const u32x4& rs, unsigned ring) {
  unsigned t;
  asm volatile("s_add_u32 m0, %3, %4\n\ts_mov_b32 %0, %5\n\tbuffer_load_dwordx4 %1, %2, %0 offen" DMAFL " lds" : "=&s"(t) : "v"(voff), "s"(rs), "s"(ring), "i"(SLOT * 1024), "i"(STEP) : "memory", "scc");
}
template <int S0, int S1, int LD> DEV void issue(const u32x4& rs, unsigned voff, unsigned ring) {
  if constexpr (S0 < S1) {
    dma<S0, S0 * 32 * LD>(voff, rs, ring);
    issue<S0 + 1, S1, LD>(rs, voff, ring);
  }
}
template <class T> DEV void PIN(T& x) { asm volatile("" : "+v"(x)); }
#define SB() __builtin_amdgcn_sched_barrier(0)
template <int OFS> DEV void ld128(u32x4& d, unsigned voff, const u32x4& rs) {
  asm volatile("buffer_load_dwordx4 %0, %1, %2, 0 offen offset:%3 sc1" : "=v"(d) : "v"(voff), "s"(rs), "i"(OFS) : "memory");
}
template <int OFS> DEV void ldx(u32x4& d, unsigned voff, const u32x4& rs) {
  asm volatile("buffer_load_dwordx4 %0, %1, %2, 0 offen offset:%3" : "=v"(d) : "v"(voff), "s"(rs), "i"(OFS) : "memory");
}
DEV void ld64(u32x2& d, unsigned voff, const u32x4& rs) {
  asm volatile("buffer_load_dwordx2 %0, %1, %2, 0 offen sc1" : "=v"(d) : "v"(voff), "s"(rs) : "memory");
}
template <int OFS> DEV void st128(const u32x4& d, unsigned voff, const u32x4& rs) {
  asm volatile("buffer_store_dwordx4 %0, %1, %2, 0 offen offset:%3 sc1\n\ts_nop 1" ::"v"(d), "v"(voff), "s"(rs), "i"(OFS) : "memory");
}
DEV void st64(const u32x2& d, unsigned voff, const u32x4& rs) {
  asm volatile("buffer_store_dwordx2 %0, %1, %2, 0 offen sc1" ::"v"(d), "v"(voff), "s"(rs) : "memory");
}
DEV u32x4 clean(const u32x4& v) {
  return (u32x4){min(v.x, SENT - 1), v.y, min(v.z, SENT - 1), v.w};
}
DEV unsigned chk(unsigned m, const u32x4& v) { return max(max(m, v.x), v.z); }
DEV void keep(const u32x4& a) { asm volatile("" ::"v"(a)); }
DEV u32x4 trfrag(unsigned addr, unsigned addr2) {
  s16x4 lo = __builtin_amdgcn_ds_read_tr16_b64_v4i16(LDSP(s16x4, addr));
  s16x4 hi = __builtin_amdgcn_ds_read_tr16_b64_v4i16(LDSP(s16x4, addr2));
  u32x2 a = __builtin_bit_cast(u32x2, lo), b = __builtin_bit_cast(u32x2, hi);
  return (u32x4){a.x, a.y, b.x, b.y};
}
DEV f32x16 mfma(const u32x4& a, const u32x4& b, const f32x16& c) {
  return __builtin_amdgcn_mfma_f32_32x32x16_bf16(__builtin_bit_cast(bf16x8, a), __builtin_bit_cast(bf16x8, b), c, 0, 0, 0);
}
DEV u32x4 packhalf(const f32x16& v, int s) {
  return (u32x4){pk_bf16(v[8 * s], v[8 * s + 1]), pk_bf16(v[8 * s + 2], v[8 * s + 3]), pk_bf16(v[8 * s + 4], v[8 * s + 5]), pk_bf16(v[8 * s + 6], v[8 * s + 7])};
}
DEV float swap32_add(float a, float b) { auto r = __builtin_amdgcn_permlane32_swap(__float_as_uint(a), __float_as_uint(b), false, false); return __uint_as_float(r[0]) + __uint_as_float(r[1]); }
DEV float swap16_add(float a, float b) { auto r = __builtin_amdgcn_permlane16_swap(__float_as_uint(a), __float_as_uint(b), false, false); return __uint_as_float(r[0]) + __uint_as_float(r[1]); }
DEV float xhalf_max(float v) { auto r = __builtin_amdgcn_permlane32_swap(__float_as_uint(v), __float_as_uint(v), false, false); return fmaxf(__uint_as_float(r[0]), __uint_as_float(r[1])); }

DEV void ln_stats(const f32x16& v, unsigned sw) {
  f32x2 s = {v[0], v[1]}, q = s * s;
#pragma unroll
  for (int r = 2; r < 16; r += 2) { f32x2 t = {v[r], v[r + 1]}; s += t; q = __builtin_elementwise_fma(t, t, q); }
  *LDSP(float, sw) = swap32_add(s.x + s.y, q.x + q.y);
}
template <int BUF> DEV void put_act(const f32x16& v, unsigned pa) {
  *LDSP(u32x4, pa + BUF) = packhalf(v, 0);
  *LDSP(u32x4, pa + BUF + 1024) = packhalf(v, 1);
}
template <int DST, int STR> DEV void ln_apply(const f32x16& v, unsigned sr, unsigned pa, float scale) {
  f32x2 st[8];
#pragma unroll
  for (int q = 0; q < 8; ++q) st[q] = *LDSP(f32x2, sr + q * STR);
  f32x2 t = ((st[0] + st[1]) + (st[2] + st[3])) + ((st[4] + st[5]) + (st[6] + st[7]));
  float mean = t.x * (1.f / 256.f);
  float var = fmaxf(fmaf(-mean, mean, t.y * (1.f / 256.f)), 0.f);
  float rstd = __builtin_amdgcn_rsqf(var + 1e-5f) * scale;
  float nb = -mean * rstd;
  f32x16 o;
#pragma unroll
  for (int r = 0; r < 16; ++r) o[r] = fmaf(v[r], rstd, nb);
  put_act<DST>(o, pa);
}
template <int S0, int S1> DEV void preread(u32x4 (&a)[16], unsigned rt) {
#pragma unroll
  for (int s = S0; s < S1; ++s) a[s] = trfrag(rt + s * 1024, rt + 16384 + s * 1024);
  SB();
}
template <int S0, int S1> DEV void preread(u32x4 (&a)[16], unsigned lo, unsigned hi) {
#pragma unroll
  for (int s = S0; s < S1; ++s) a[s] = trfrag(lo + s * 1024, hi + s * 1024);
  SB();
}
template <int S0, int S1> DEV void pin(u32x4 (&a)[16]) {
#pragma unroll
  for (int s = S0; s < S1; ++s) asm volatile("" : "+v"(a[s]));
}
template <int N> struct IC { static constexpr int v = N; };
template <int I, int N, class F> DEV void sfor(F&& f) { if constexpr (I < N) { f(IC<I>{}); sfor<I + 1, N>(f); } }
template <bool SWAP, bool TRB, class F> DEV f32x16 gemm(u32x4 (&a)[16], unsigned bb, f32x16 acc, F&& hook) {
  u32x4 b[16];
  constexpr int AH = TRB ? 3 : 4;
#pragma unroll
  for (int s = 0; s < AH; ++s) b[s] = TRB ? trfrag(bb + (s << 10), bb + (s << 10) + 512) : *LDSP(u32x4, bb + (s << 10));
  SB();
  sfor<0, 16>([&](auto I) {
    constexpr int s = I.v;
    hook(I);
    if constexpr (s + AH < 16) b[s + AH] = TRB ? trfrag(bb + ((s + AH) << 10), bb + ((s + AH) << 10) + 512) : *LDSP(u32x4, bb + ((s + AH) << 10));
    SB();
    acc = SWAP ? mfma(b[s], a[s], acc) : mfma(a[s], b[s], acc);
    SB();
  });
  return acc;
}
DEV void wait_lds() { asm volatile("s_waitcnt lgkmcnt(0)" ::: "memory"); }
DEV void bar_only() { asm volatile("s_barrier" ::: "memory"); }
DEV void poll(unsigned voff, const u32x2& tag, const u32x4& rs) {
#pragma unroll 1
  for (int spins = 0; spins < SPIN_CAP; ++spins) {
    u32x2 v; ld64(v, voff, rs);
    asm volatile("s_waitcnt vmcnt(0)" : "+v"(v)::"memory");
    if (__all(v.x == tag.x && v.y == tag.y)) break;
    __builtin_amdgcn_s_sleep(1);
  }
}
template <int R0, int R1> DEV void silu2(f32x16& v) {
#pragma unroll
  for (int r = R0; r < R1; ++r) v[r] *= sig2(v[r]);
  asm volatile("" : "+v"(v));
}
#define STAMP(i)
#define FS(i)

__global__ void __launch_bounds__(512, 2) conformer_kernel(Params p_) {
  STAMP(0);
  const int wg = blockIdx.x;
  const int j = wg & 3, tile = ((wg >> 2) & 1) * 32 + (wg >> 3), cu = wg >> 3;
  const int b = tile >> 5, ti = tile & 31;
  const int w = __builtin_amdgcn_readfirstlane(threadIdx.x >> 6);
  const __attribute__((address_space(4))) Params* kp = (const __attribute__((address_space(4))) Params*)__builtin_amdgcn_kernarg_segment_ptr();
  const u16* tptr = kp->tt[w].ptr;
  unsigned trs = kp->tt[w].rs, tjm = kp->tt[w].jm, tc2 = kp->tt[w].c2, tcm = kp->tt[w].cm;
  struct { const u16 *x, *w1a, *w1b, *wqkv, *wo, *wp1, *dw, *wp2, *w2a, *w2b; u16* out; unsigned char* ws; } p =
      {kp->x, kp->w1a, kp->w1b, kp->wqkv, kp->wo, kp->wp1, kp->dw, kp->wp2, kp->w2a, kp->w2b, kp->out, kp->ws};
  asm volatile("" : "+s"(tptr), "+s"(trs), "+s"(tjm), "+s"(tc2), "+s"(tcm), "+s"(p.x), "+s"(p.w1a), "+s"(p.w1b), "+s"(p.wqkv), "+s"(p.wo),
               "+s"(p.wp1), "+s"(p.dw), "+s"(p.wp2), "+s"(p.w2a), "+s"(p.w2b), "+s"(p.out), "+s"(p.ws));
  FS(13);
  const int nvalid = ti == 31 ? 8 : 32;
  const unsigned ring = RING + w * 16384;
  const u32x4 rws = mkrs(p.ws, WS_USED);
  int lane, h, tok;
  unsigned trofs, rt, crow, ccol, v1024, v256, l16, pa, sa, sw, sr, oGLUst;
  auto lane0 = [&]() {
    asm volatile("v_mbcnt_lo_u32_b32 %0, -1, 0\n\tv_mbcnt_hi_u32_b32 %0, -1, %0" : "=v"(lane));
    h = lane >> 5; tok = lane & 31;
  };
  auto lanevals = [&]() {
    trofs = (h * 4 + ((lane & 15) >> 2)) * 64 + ((lane >> 4) & 1) * 32 + (lane & 3) * 8;
    rt = RING + (w >> 1) * 32768 + (h * 4 + ((lane & 15) >> 2)) * 128 + (((w ^ (lane >> 3)) & 1) * 64) + ((lane >> 4) & 1) * 32 + (lane & 3) * 8;
    crow = (lane >> 3) + 8 * (w & 1);
    ccol = ((lane & 7) ^ ((lane >> 2) & 4)) * 16;
    v1024 = crow * 2048 + 512 * j + (w >> 1) * 128 + ccol; v256 = crow * 512 + (w >> 1) * 128 + ccol;
    l16 = lane * 16; pa = l16 + w * 2048; sr = tok * 8; sa = sr + h * 4; sw = sa + w * 256;
    oGLUst = OFF_GLU + (unsigned)(tile * TS + ((lane >> 4) & 1) * 8192 + (2 * j + (w >> 1)) * 1024 + (lane & 1) * 512 + ((w & 1) * 8 + ((lane & 15) >> 1)) * 32 + 16 * h);
    PIN(rt); PIN(v1024); PIN(v256); PIN(l16); PIN(pa); PIN(sw); PIN(sr); PIN(oGLUst);
  };
  lane0();
  const u32x4 sent = {SENT, SENT, SENT, SENT};
  auto flagoff = [&](int hop, int t, int jj, int wave) { return OFF_FLAG + (unsigned)((((hop * 64 + t) * 4 + jj) * 8 + wave) * 128); };
  const u32x4 r1a = mkrs(p.w1a, 524288), r1b = mkrs(p.w1b + 65536 * j, 131072);
  const f32x16 zero16 = {0};
  auto nohook = [&](auto) {};
  auto touch = [&]() { return (8 * cu + (lane >> 2)) * trs + j * tjm + min((unsigned)lane & 3u, tcm) * tc2; };
  f32x16 xres;
  u32x2 tag;
  u32x4 wr[16], wr2[16];
  unsigned oP1, junk = 0, junk4 = 0, junk5 = 0;
  {
    const u32x4 rx = mkrs(p.x, 2000u * 512u);
    const unsigned xo = tok < nvalid ? (unsigned)((b * 1000 + ti * 32 + tok) * 512 + (32 * w + 16 * h) * 2) : OOB;
    u32x4 x0, x1;
    ldx<0>(x0, xo, rx); ldx<16>(x1, xo, rx);
    if (lane < 32 && w < T1W) asm volatile("global_load_dword %0, %1, %2" : "=v"(junk) : "v"(touch()), "s"(tptr) : "memory");
    ld64(tag, OFF_EPOCH + b * 256, rws);
    SB();
    FS(14);
    lanevals();
    oP1 = OFF_P1 + (unsigned)(tile * TS) + pa;
    st128<0>(sent, oP1 + j * 16384, rws); st128<1024>(sent, oP1 + j * 16384, rws);
    if (NOWAIT && w > 0 && w < T1W) wait_vm<4>(); else wait_vm<3>();
    asm volatile("" : "+v"(x0), "+v"(x1)::"memory");
    FS(15);
    bar_only();
    issue<0, 4, 1024>(r1a, v1024, ring);
    SB();
    auto a0 = __builtin_amdgcn_permlane32_swap(x0.x, x0.z, false, false), a1 = __builtin_amdgcn_permlane32_swap(x0.y, x0.w, false, false);
    auto c0 = __builtin_amdgcn_permlane32_swap(x1.x, x1.z, false, false), c1 = __builtin_amdgcn_permlane32_swap(x1.y, x1.w, false, false);
    const unsigned xv[8] = {a0[0], a1[0], c0[0], c1[0], a0[1], a1[1], c0[1], c1[1]};
#pragma unroll
    for (int r = 0; r < 8; ++r) { xres[2 * r] = bf_lo(xv[r]); xres[2 * r + 1] = bf_hi(xv[r]); }
    STAMP(1);
    ln_stats(xres, BUFB + sw);
    issue<4, 8, 1024>(r1a, v1024, ring);
    bar();
    ln_apply<BUFA, 256>(xres, BUFB + sr, pa, LOG2E);
    SB();
    issue<8, 16, 1024>(r1a, v1024, ring);
    asm volatile("s_waitcnt vmcnt(8) lgkmcnt(0)" : "+v"(tag), "+v"(junk)::"memory");
    bar_only();
    preread<0, 4>(wr, rt);
    u64 e = (((u64)tag.y << 32) | tag.x) + 1;
    tag = (u32x2){(unsigned)e, (unsigned)(e >> 32)};
    if (w < 4) __builtin_amdgcn_s_setprio(2);
  }
  STAMP(2);
  auto late = [&]() {
    if (lane < 32 && w >= T1W && w < 4) asm volatile("global_load_dword %0, %1, %2" : "=v"(junk) : "v"(touch()), "s"(tptr) : "memory");
  };
  {
    f32x16 acc = gemm<false, false>(wr, BUFA + l16, zero16, [&](auto I) {
      constexpr int s = I.v;
      if constexpr (s == 0) preread<4, 8>(wr, rt);
      if constexpr (s == 4) { pin<4, 8>(wr); wait_vm<0>(); bar_only(); if (w == 7) st64(tag, lane == 0 ? flagoff(0, tile, j, 0) : OOB, rws); late(); issue<0, 2, 256>(r1b, v256, ring); preread<8, 12>(wr, rt); }
      if constexpr (s == 8) preread<12, 16>(wr, rt);
      if constexpr ((s & 1) && s > 4) issue<s / 2, s / 2 + 1, 256>(r1b, v256, ring);
      if constexpr (s == 15) { pin<15, 16>(wr); bar_only(); }
    });
    FS(10);
    sfor<0, 8>([&](auto I) { issue<8 + I.v, 9 + I.v, 256>(r1b, v256, ring); silu2<2 * I.v, 2 * I.v + 2>(acc); SB(); });
    FS(11);
    put_act<BUFB>(acc, pa);
    asm volatile("s_waitcnt vmcnt(8) lgkmcnt(0)" ::: "memory");
    PIN(junk);
    FS(12);
    if (w < 4) { st128<0>(sent, OFF_ATT + (unsigned)(tile * TS + j * 4096 + w * 1024) + l16, rws); st128<0>(sent, oGLUst, rws); }
  }
  bar_only();
  preread<0, 4>(wr, rt);
  STAMP(3);
  const u32x4 rq = mkrs(p.wqkv, 393216), ro = mkrs(p.wo, 131072);
  {
    u32x2 fl;
    unsigned fo = flagoff(0, tile, (j + 1 + (lane & 1) + ((lane >> 1) & 1)) & 3, 0);
    f32x16 acc = gemm<false, false>(wr, BUFB + l16, zero16, [&](auto I) {
      constexpr int s = I.v;
      if constexpr (s == 0) preread<4, 8>(wr, rt);
      if constexpr (s == 5) { wait_vm<0>(); bar_only(); preread<8, 12>(wr, rt); }
      if constexpr (s == 8) preread<12, 16>(wr, rt);
      if constexpr (s == 6) ld64(fl, fo, rws);
    });
#pragma unroll
    for (int r = 0; r < 16; ++r) acc[r] *= 0.5f / LOG2E;
    st128<0>(clean(packhalf(acc, 0)), oP1 + j * 16384, rws);
    st128<1024>(clean(packhalf(acc, 1)), oP1 + j * 16384, rws);
    STAMP(4);
    unsigned v768 = crow * 1536 + (w < 4 ? 512 + (w >> 1) * 512 : 0) + 128 * j + ccol;
    PIN(v768);
    bar_only();
    if (w < 6) { issue<0, H1DMA, 768>(rq, v768, ring); wait_vm<H1DMA + 2>(); } else wait_vm<2>();
    PIN(fl);
    STAMP(5);
    xres += acc;
    PIN(xres);
    u32x4 idf[2];
    {
      const int n = ((tok >> 2) & 1) == h ? 2 * (tok >> 3) + ((tok >> 1) & 1) : 8;
      const unsigned one = 0x3F80u << (16 * (tok & 1));
#pragma unroll
      for (int q = 0; q < 8; ++q) idf[q >> 2][q & 3] = n == q ? one : 0u;
      PIN(idf[0]); PIN(idf[1]);
    }
    unsigned o0 = oP1 + ((j + 1) & 3) * 16384, o1 = oP1 + ((j + 2) & 3) * 16384, o2 = oP1 + ((j + 3) & 3) * 16384;
    PIN(o0); PIN(o1); PIN(o2);
    bar_only();
    if (!__all(fl.x == tag.x && fl.y == tag.y)) poll(fo, tag, rws);
    u32x4 pv[6];
#pragma unroll 1
    for (int spins = 0; spins < SPIN_CAP; ++spins) {
      ld128<0>(pv[0], o0, rws); ld128<1024>(pv[1], o0, rws); ld128<0>(pv[2], o1, rws); ld128<1024>(pv[3], o1, rws); ld128<0>(pv[4], o2, rws); ld128<1024>(pv[5], o2, rws);
      asm volatile("s_waitcnt vmcnt(0)" : "+v"(pv[0]), "+v"(pv[1]), "+v"(pv[2]), "+v"(pv[3]), "+v"(pv[4]), "+v"(pv[5])::"memory");
      if (__all(chk(chk(chk(chk(chk(chk(0, pv[0]), pv[1]), pv[2]), pv[3]), pv[4]), pv[5]) != SENT)) break;
    }
    STAMP(6);
#pragma unroll
    for (int q = 0; q < 6; ++q) xres = mfma(idf[q & 1], pv[q], xres);
    SB();
    if (lane < 32 && w >= 4) asm volatile("global_load_dword %0, %1, %2" : "=v"(junk4) : "v"(touch()), "s"(tptr) : "memory");
    if (lane < 4 && w == 5) asm volatile("global_load_dword %0, %1, %2" : "=v"(junk5) : "v"((unsigned)(min(cu, 30) * 512 + lane * 128)), "s"(p.dw) : "memory");
    STAMP(7);
  }
  ln_stats(xres, BUFA + sw);
  bar();
  ln_apply<BUFB, 256>(xres, BUFA + sr, pa, 1.f);
  wait_lds();
  if (w < 6) preread<0, 6>(wr, rt);
  bar_only();
  STAMP(8);
  unsigned oKV = OFF_KV + (unsigned)((b * 4 + j) * 32 * 8192) + l16;
  if (w < 4) {
    f32x16 acc;
    unsigned o = oKV + ti * 8192 + w * 2048;
    auto hk = [&](auto I) { if constexpr (I.v == 1) PIN(o); if constexpr (I.v < 10) preread<I.v + 6, I.v + 7>(wr, rt); if constexpr (I.v == 12) { pin<15, 16>(wr); bar_only(); } };
    if (w >= 2) acc = gemm<true, false>(wr, BUFB + l16, zero16, hk); else acc = gemm<false, false>(wr, BUFB + l16, zero16, hk);
    st128<0>(packhalf(acc, 0), o, rws);
    st128<1024>(packhalf(acc, 1), o, rws);
    issue<0, 8, 256>(ro, v256, ring);
    wait_vm<8>();
  } else if (w < 6) {
    f32x16 acc = gemm<false, false>(wr, BUFB + l16, zero16, [&](auto I) { if constexpr (I.v < 10) preread<I.v + 6, I.v + 7>(wr, rt); if constexpr (I.v == 12) { pin<15, 16>(wr); bar_only(); } });
#pragma unroll
    for (int r = 0; r < 16; ++r) acc[r] *= 0.125f * LOG2E;
    put_act<BUFA>(acc, pa - 8192);
    issue<0, 8, 256>(ro, v256, ring);
  } else {
    issue<0, 15, 256>(ro, v256, ring);
    bar_only();
  }
  unsigned fo2 = lane == 0 ? flagoff(1, tile, j, 0) : OOB, to2 = OFF_KV + (unsigned)(((b * 4 + j) * 32 + ti) * 8192 + (w & 1) * 4096 + lane * 64);
  PIN(fo2); PIN(to2);
  STAMP(9);
  bar();
  unsigned junk3 = 0;
  if (w == 7) st64(tag, fo2, rws);
  if (w >= 6) asm volatile("buffer_load_dword %0, %1, %2, 0 offen sc1" : "=v"(junk3) : "v"(to2), "s"(rws) : "memory");
  u32x4 qf[4];
#pragma unroll
  for (int s = 0; s < 4; ++s) qf[s] = *LDSP(u32x4, BUFA + s * 1024 + l16);
  if (w < 6) issue<8, 15, 256>(ro, v256, ring); else __builtin_amdgcn_s_sleep(H2SLEEP);
  asm volatile("" : "+v"(qf[0]), "+v"(qf[1]), "+v"(qf[2]), "+v"(qf[3]));
  oKV += 4 * w * 8192;
  PIN(oKV);
  bar();
  poll(flagoff(1, b * 32 + 4 * w + (lane & 3), j, 0), tag, rws);
  PIN(junk3); PIN(junk4); PIN(junk5);
  STAMP(10);
  unsigned oP5st;
  {
    const __amdgpu_buffer_rsrc_t rkv = __builtin_amdgcn_make_buffer_rsrc(p.ws, 0, WS_USED, 0x00020000);
    u32x4 kf[4][4], vf[4][4];
#pragma unroll
    for (int t = 0; t < 4; ++t)
#pragma unroll
      for (int s = 0; s < 4; ++s) kf[t][s] = __builtin_amdgcn_raw_buffer_load_b128(rkv, oKV + s * 1024, t * 8192, 16);
    SB();
    f32x16 sc[4];
    float mx = -INFINITY;
#pragma unroll
    for (int t = 0; t < 4; ++t) {
      f32x16 a = {0};
#pragma unroll
      for (int s = 0; s < 4; ++s) a = mfma(kf[t][s], qf[s], a);
      SB();
#pragma unroll
      for (int s = 0; s < 4; ++s) vf[t][s] = __builtin_amdgcn_raw_buffer_load_b128(rkv, oKV + s * 1024, t * 8192 + 4096, 16);
      SB();
      if (t == 3 && w == 7) {
#pragma unroll
        for (int r = 4; r < 16; ++r) a[r] = -INFINITY;
      }
      sc[t] = a;
      if (t > 0) {
#pragma unroll
        for (int r = 0; r < 16; r += 2) mx = fmaxf(fmaxf(mx, sc[t - 1][r]), sc[t - 1][r + 1]);
        SB();
      }
    }
#pragma unroll
    for (int r = 0; r < 16; r += 2) mx = fmaxf(fmaxf(mx, sc[3][r]), sc[3][r + 1]);
    mx = xhalf_max(mx);
    const f32x2 mx2 = {mx, mx};
    f32x2 s2 = {0.f, 0.f};
    f32x16 o0 = {0}, o1 = {0};
#pragma unroll
    for (int t = 0; t < 4; ++t) {
      unsigned pk[8];
#pragma unroll
      for (int r = 0; r < 8; ++r) {
        f32x2 d = (f32x2){sc[t][2 * r], sc[t][2 * r + 1]} - mx2;
        d = (f32x2){__builtin_amdgcn_exp2f(d.x), __builtin_amdgcn_exp2f(d.y)};
        s2 += d;
        pk[r] = pk_bf16(d.x, d.y);
      }
      const u32x4 p0 = {pk[0], pk[1], pk[2], pk[3]}, p1 = {pk[4], pk[5], pk[6], pk[7]};
      o0 = mfma(vf[t][0], p0, o0); o1 = mfma(vf[t][2], p0, o1);
      o0 = mfma(vf[t][1], p1, o0); o1 = mfma(vf[t][3], p1, o1);
      SB();
    }
    lane0(); lanevals();
    float sum = s2.x + s2.y;
    sum = swap32_add(sum, sum);
    *LDSP(float, ring + 15 * 1024 + sa) = h ? sum : mx;
    SB();
    put_act<BUFA>(o0, pa + w * 2048);
    put_act<BUFA + 2048>(o1, pa + w * 2048);
  }
  STAMP(11);
  bar();
  {
    f32x2 L[8];
    u32x2 pv[8];
#pragma unroll
    for (int q = 0; q < 8; ++q) L[q] = *LDSP(f32x2, RING + q * 16384 + 15 * 1024 + (lane >> 1) * 8);
#pragma unroll
    for (int q = 0; q < 8; ++q) pv[q] = *LDSP(u32x2, BUFA + q * 4096 + w * 512 + lane * 8);
    SB();
    float lm = fmaxf(fmaxf(fmaxf(L[0].x, L[1].x), fmaxf(L[2].x, L[3].x)), fmaxf(fmaxf(L[4].x, L[5].x), fmaxf(L[6].x, L[7].x)));
    float om = 0.f;
    f32x2 a0 = {0, 0}, a1 = {0, 0};
#pragma unroll
    for (int q = 0; q < 8; ++q) {
      float f = __builtin_amdgcn_exp2f(L[q].x - lm);
      om = fmaf(f, L[q].y, om);
      const f32x2 f2 = {f, f};
      a0 = __builtin_elementwise_fma(f2, (f32x2){bf_lo(pv[q].x), bf_hi(pv[q].x)}, a0);
      a1 = __builtin_elementwise_fma(f2, (f32x2){bf_lo(pv[q].y), bf_hi(pv[q].y)}, a1);
    }
    om = __builtin_amdgcn_rcpf(om);
    a0 *= om; a1 *= om;
    st64((u32x2){min(pk_bf16(a0.x, a0.y), SENT - 1), pk_bf16(a1.x, a1.y)}, OFF_ATT + (unsigned)(tile * TS + j * 4096 + w * 512 + lane * 8), rws);
  }
  preread<0, 15>(wr, rt);
  oP5st = ((tok >> 3) == j) ? OOB : OFF_P5 + (unsigned)(tile * TS + ((tok >> 3) * 8 + w) * 2048 + j * 256 + h * 128 + (tok & 7) * 16);
  st128<0>(sent, oP5st, rws); st128<1024>(sent, oP5st, rws);
  bar();
  STAMP(12);
  issue<15, 16, 256>(ro, v256, ring);
  if (ti == 0 && j == 0 && w == 4) st64(tag, lane == 0 ? OFF_EPOCH + b * 256 : OOB, rws);
  const u32x4 rp1 = mkrs(p.wp1, 262144), rp2 = mkrs(p.wp2, 131072), rdw = mkrs(p.dw, 31 * 512);
  const u32x4 r2a = mkrs(p.w2a, 524288), r2b = mkrs(p.w2b + 65536 * j, 131072);
  const unsigned v512 = ((lane >> 2) * 512 + 64 * j + 16 * w + (lane & 1) * 8 + ((lane >> 1) & 1) * 256) * 2;
  pin<0, 15>(wr);
  { const unsigned vd = (lane >> 2) * 512 + (lane & 3) * 16 + 64 * w; dma<0, 0>(vd, rdw, BUFB + w * 2048); dma<1, 8192>(vd, rdw, BUFB + w * 2048); }
  if (w < 4) issue<0, 4, 512>(rp1, v512, ring); else issue<0, 4, 1024>(r2a, v1024, ring);
  unsigned z31 = BUFB + w * 2048 + 31 * 64 + (lane & 15) * 4, sw3 = ring + 3 * 1024 + sa, sr3 = RING + 3 * 1024 + sr;
  unsigned r1lo = w < 4 ? ring + trofs : rt, r1hi = r1lo + (w < 4 ? 512 : 16384);
  PIN(r1lo); PIN(r1hi); PIN(trofs); PIN(z31); PIN(sw3); PIN(sr3);
  {
    const unsigned o = OFF_ATT + (unsigned)(tile * TS) + pa;
    u32x4 a0, a1;
    ld128<0>(a0, o, rws); ld128<1024>(a1, o, rws);
    bar_only();
    if (w < 4) issue<4, 12, 512>(rp1, v512, ring); else issue<4, 12, 1024>(r2a, v1024, ring);
    asm volatile("s_waitcnt vmcnt(8)" : "+v"(a0), "+v"(a1)::"memory");
    *LDSP(unsigned, z31) = 0;
#pragma unroll 1
    for (int spins = 0; spins < SPIN_CAP && !__all(chk(chk(0, a0), a1) != SENT); ++spins) {
      ld128<0>(a0, o, rws); ld128<1024>(a1, o, rws);
      asm volatile("s_waitcnt vmcnt(0)" : "+v"(a0), "+v"(a1)::"memory");
    }
    *LDSP(u32x4, BUFA + pa) = a0;
    *LDSP(u32x4, BUFA + pa + 1024) = a1;
  }
  bar();
  STAMP(13);
  xres = gemm<false, false>(wr, BUFA + l16, xres, [&](auto I) {
    constexpr int s = I.v;
    if constexpr (s == 1) { preread<15, 16>(wr, rt); preread<0, 4>(wr2, r1lo, r1hi); }
    if constexpr (s == 1 || s == 3 || s == 5) { if (w < 4) issue<12 + s / 2, 13 + s / 2, 512>(rp1, v512, ring); else issue<12 + s / 2, 13 + s / 2, 1024>(r2a, v1024, ring); }
    if constexpr (s == 8) { pin<15, 16>(wr); pin<0, 4>(wr2); wait_vm<3>(); bar_only(); preread<4, 8>(wr2, r1lo, r1hi); }
    if constexpr (s == 9) { if (w < 4) issue<15, 16, 512>(rp1, v512, ring); else issue<15, 16, 1024>(r2a, v1024, ring); }
    if constexpr (s == 12) preread<8, 12>(wr2, r1lo, r1hi);
    if constexpr (s == 11 || s == 13 || s == 15) issue<s / 2 - 5, s / 2 - 4, 256>(rp2, v256, ring);
  });
  ln_stats(xres, sw3);
  wait_vm<3>();
  bar();
  pin<4, 12>(wr2);
  const unsigned hw = w >> 2, ringh = RING + (w & 3) * 16384;
  issue<4, 6, 256>(rp2, v256 + hw * (4 * 8192 - 256), ringh + hw * 4096);
  ln_apply<BUFA, 16384>(xres, sr3, pa, 1.f);
  SB();
  preread<12, 16>(wr2, r1lo, r1hi);
  issue<6, 8, 256>(rp2, v256 + hw * (4 * 8192 - 256), ringh + hw * 4096);
  bar();
  STAMP(14);
  pin<12, 16>(wr2);
  u32x2 tw[2][9];
  auto build_tw = [&]() {
  {
    const int c = lane >> 2, i = lane & 3;
    const unsigned tc = BUFB + w * 2048 + c * 4, tb = tc - (i + 1) * 64;
    unsigned t[36];
#pragma unroll
    for (int n = 0; n < 36; ++n) {
      if (n == 0 || n == 35) t[n] = 0;
      else if (n < 4 || n > 32) t[n] = *LDSP(unsigned, tc + min((unsigned)(n - 1 - i), 31u) * 64);
      else t[n] = *LDSP(unsigned, tb + n * 64);
    }
#pragma unroll
    for (int u = 0; u < 9; ++u) {
      tw[0][u] = (u32x2){__builtin_amdgcn_perm(t[4 * u + 1], t[4 * u], 0x05040100u), __builtin_amdgcn_perm(t[4 * u + 3], t[4 * u + 2], 0x05040100u)};
      tw[1][u] = (u32x2){__builtin_amdgcn_perm(t[4 * u + 1], t[4 * u], 0x07060302u), __builtin_amdgcn_perm(t[4 * u + 3], t[4 * u + 2], 0x07060302u)};
      PIN(tw[0][u]); PIN(tw[1][u]);
    }
  }
  };
  if (w < 4) {
    f32x16 acc = gemm<true, false>(wr2, BUFA + l16, zero16, [&](auto I) {
      if constexpr (I.v == 1) { issue<3, 4, 256>(rp2, v256, ring); issue<12, 16, 256>(rp2, v256, ring); }
    });
    float g[8];
    const int e = (lane >> 4) & 1;
#pragma unroll
    for (int r = 0; r < 8; ++r) {
      auto sw = __builtin_amdgcn_permlane16_swap(__float_as_uint(acc[r]), __float_as_uint(acc[r + 8]), false, false);
      g[r] = __uint_as_float(sw[0]) * sig2(LOG2E * __uint_as_float(sw[1]));
    }
    if (ti == 31) {
#pragma unroll
      for (int r = 0; r < 8; ++r) g[r] = ((r & 3) + 8 * (r >> 2) + 16 * e + 4 * h < 8) ? g[r] : 0.f;
    }
    auto sx = __builtin_amdgcn_permlane32_swap(pk_bf16(g[0], g[1]), pk_bf16(g[4], g[5]), false, false);
    auto sy = __builtin_amdgcn_permlane32_swap(pk_bf16(g[2], g[3]), pk_bf16(g[6], g[7]), false, false);
    st128<0>(clean((u32x4){sx[0], sy[0], sx[1], sy[1]}), oGLUst, rws);
    wait_vm<1>();
  } else {
    issue<3, 16, 256>(rp2, v256, ring);
    wait_vm<0>();
  }
  STAMP(15);
  bar_only();
  preread<0, 16>(wr, rt);
  bar();
  pin<0, 16>(wr);
  if (w < 4) issue<0, 6, 1024>(r2a, v1024, ring); else issue<0, 4, 256>(r2b, v256, ring);
  build_tw();
  {
    const int c = lane >> 2, jj = lane & 3;
    unsigned o0 = OFF_GLU + (unsigned)(tile * TS + (jj < 2 ? 8192 - TS : 0) + w * 1024 + c * 32 + (jj & 1) * 16), o1 = o0 + TS;
    if (jj < 2 && ti == 0) o0 = OOB;
    if (jj >= 2 && ti == 31) o1 = OOB;
    unsigned pc = BUFB + w * 2048 + c * 128 + jj * 16;
    PIN(o0); PIN(o1); PIN(pc);
    u32x4 c0[2], c1[2];
#pragma unroll 1
    for (int spins = 0; spins < SPIN_CAP; ++spins) {
      ld128<0>(c0[0], o0, rws); ld128<0>(c1[0], o1, rws); ld128<512>(c0[1], o0, rws); ld128<512>(c1[1], o1, rws);
      asm volatile("s_waitcnt vmcnt(0)" : "+v"(c0[0]), "+v"(c1[0]), "+v"(c0[1]), "+v"(c1[1])::"memory");
      if (__all(chk(chk(chk(chk(0, c0[0]), c1[0]), c0[1]), c1[1]) != SENT)) break;
    }
    STAMP(16);
    issue<6, 9, 1024>(r2a, v1024 + hw * (5 * 32768 - 256), ringh + hw * 5120);
    f32x4 d[2][2];
#pragma unroll
    for (int g = 0; g < 2; ++g) {
      u32x4 ch[5];
      ch[0] = c0[g]; ch[4] = c1[g];
#pragma unroll
      for (int dd = 0; dd < 4; ++dd) {
        ch[1][dd] = __builtin_amdgcn_mov_dpp(jj < 1 ? c1[g][dd] : c0[g][dd], 0x39, 0xf, 0xf, false);
        ch[2][dd] = __builtin_amdgcn_mov_dpp(jj < 2 ? c1[g][dd] : c0[g][dd], 0x4e, 0xf, 0xf, false);
        ch[3][dd] = __builtin_amdgcn_mov_dpp(jj < 3 ? c1[g][dd] : c0[g][dd], 0x93, 0xf, 0xf, false);
      }
      f32x4 d0 = {0, 0, 0, 0}, d1 = {0, 0, 0, 0};
#pragma unroll
      for (int u = 0; u < 9; ++u) {
        u32x2 q0 = {ch[u >> 1][2 * (u & 1)], ch[u >> 1][2 * (u & 1) + 1]};
        u32x2 q1 = {ch[(u + 1) >> 1][2 * ((u + 1) & 1)], ch[(u + 1) >> 1][2 * ((u + 1) & 1) + 1]};
        d0 = __builtin_amdgcn_mfma_f32_4x4x4bf16_1k(__builtin_bit_cast(s16x4, tw[g][u]), __builtin_bit_cast(s16x4, q0), d0, 0, 0, 0);
        d1 = __builtin_amdgcn_mfma_f32_4x4x4bf16_1k(__builtin_bit_cast(s16x4, tw[g][u]), __builtin_bit_cast(s16x4, q1), d1, 0, 0, 0);
      }
      d[g][0] = d0; d[g][1] = d1;
      if (g == 0) issue<9, 11, 1024>(r2a, v1024 + hw * (5 * 32768 - 256), ringh + hw * 5120);
    }
#pragma unroll
    for (int g = 0; g < 2; ++g) {
#pragma unroll
      for (int r = 0; r < 4; ++r) { d[g][0][r] *= sig2(LOG2E * d[g][0][r]); d[g][1][r] *= sig2(LOG2E * d[g][1][r]); }
      *LDSP(u32x4, pc + g * 64) = (u32x4){pk_bf16(d[g][0][0], d[g][0][1]), pk_bf16(d[g][0][2], d[g][0][3]), pk_bf16(d[g][1][0], d[g][1][1]), pk_bf16(d[g][1][2], d[g][1][3])};
    }
    wait_vm<0>();
    if (w >= 4) issue<4, 8, 256>(r2b, v256, ring);
  }
  bar();
  STAMP(17);
  xres = gemm<false, true>(wr, trofs + BUFB, xres, [&](auto I) {
    constexpr int s = I.v;
    if constexpr (s & 1) { if (w < 4) preread<s - 1, s + 1>(wr2, rt); else issue<8 + s / 2, 9 + s / 2, 256>(r2b, v256, ring); }
  });
  ln_stats(xres, BUFA + sw);
  bar();
  pin<0, 16>(wr2);
  const unsigned vh = v256 + hw * (8 * 8192 - 256), rh = ringh + hw * 8192;
  issue<0, 2, 256>(r2b, vh, rh);
  ln_apply<BUFB, 256>(xres, BUFA + sr, pa, LOG2E);
  SB();
  issue<2, 4, 256>(r2b, vh, rh);
  bar();
  STAMP(18);
  {
    f32x16 acc = gemm<false, false>(wr2, BUFB + l16, zero16, [&](auto I) {
      constexpr int s = I.v;
      if constexpr ((s & 1) && s < 8) issue<4 + s / 2, 5 + s / 2, 256>(r2b, vh, rh);
    });
    silu2<0, 8>(acc);
    wait_vm<0>(); bar_only();
    preread<0, 8>(wr, rt);
    silu2<8, 16>(acc);
    put_act<BUFA>(acc, pa);
  }
  bar();
  STAMP(19);
  {
    unsigned o = ((lane >> 4) == j) ? OOB : OFF_P5 + (unsigned)(tile * TS + j * 16384) + pa;
    unsigned oa = ring + h * 128 + (tok & 7) * 16;
    f32x16 acc = gemm<false, false>(wr, BUFA + l16, zero16, [&](auto I) {
      if constexpr (I.v == 1) { PIN(o); PIN(oa); }
      if constexpr (I.v < 8) preread<I.v + 8, I.v + 9>(wr, rt);
    });
#pragma unroll
    for (int r = 0; r < 16; ++r) acc[r] *= 0.5f / LOG2E;
    st128<0>(clean(packhalf(acc, 0)), oP5st, rws);
    st128<1024>(clean(packhalf(acc, 1)), oP5st, rws);
    STAMP(20);
    xres += acc;
    if ((tok >> 3) == j) {
#pragma unroll
      for (int g = 0; g < 4; ++g)
        *LDSP(f32x4, oa + ((g >> 1) + 2 * (g & 1)) * 256) = (f32x4){xres[4 * g], xres[4 * g + 1], xres[4 * g + 2], xres[4 * g + 3]};
    }
    wait_lds();
    f32x4 own = *LDSP(f32x4, ring + l16);
    const int otok = 8 * j + (lane & 7), qg = lane >> 4;
    unsigned oo = otok < nvalid ? (unsigned)(((b * 1000 + ti * 32 + otok) * 256 + 32 * w + 8 * ((qg >> 1) + 2 * (qg & 1)) + 4 * ((lane >> 3) & 1)) * 2) : OOB;
    unsigned sf = BUFB + (lane & 7) * 8, sg = (lane & 24) == 0 ? sf + w * 64 + h * 4 : BUFB + 1024;
    const u32x4 rout = mkrs(p.out, 2000u * 512u);
    PIN(own); PIN(oo); PIN(sf); PIN(sg);
    __builtin_amdgcn_s_sleep(P5SLEEP);
    u32x4 a0, a1;
#pragma unroll 1
    for (int spins = 0; spins < SPIN_CAP; ++spins) {
      ld128<0>(a0, o, rws); ld128<1024>(a1, o, rws);
      asm volatile("s_waitcnt vmcnt(0)" : "+v"(a0), "+v"(a1)::"memory");
      if (__all(chk(chk(0, a0), a1) != SENT)) break;
    }
    STAMP(21);
    float t8[8], v4[4];
#pragma unroll
    for (int r = 0; r < 2; ++r) {
      auto s0 = __builtin_amdgcn_permlane32_swap(a0[r], a0[r + 2], false, false), s1 = __builtin_amdgcn_permlane32_swap(a1[r], a1[r + 2], false, false);
      t8[2 * r] = bf_lo(s0[0]) + bf_lo(s0[1]); t8[2 * r + 1] = bf_hi(s0[0]) + bf_hi(s0[1]);
      t8[4 + 2 * r] = bf_lo(s1[0]) + bf_lo(s1[1]); t8[4 + 2 * r + 1] = bf_hi(s1[0]) + bf_hi(s1[1]);
    }
#pragma unroll
    for (int r = 0; r < 4; ++r) v4[r] = swap16_add(t8[r], t8[4 + r]) + own[r];
    float s = (v4[0] + v4[1]) + (v4[2] + v4[3]);
    float q = fmaf(v4[0], v4[0], v4[1] * v4[1]) + fmaf(v4[2], v4[2], v4[3] * v4[3]);
    float z = swap32_add(s, q);
    z = swap16_add(z, z);
    z += __uint_as_float(__builtin_amdgcn_mov_dpp(__float_as_uint(z), 0x128, 0xf, 0xf, false));
    *LDSP(float, sg) = z;
    bar();
    f32x2 st[8];
#pragma unroll
    for (int pp = 0; pp < 8; ++pp) st[pp] = *LDSP(f32x2, sf + pp * 64);
    f32x2 t = ((st[0] + st[1]) + (st[2] + st[3])) + ((st[4] + st[5]) + (st[6] + st[7]));
    float mean = t.x * (1.f / 256.f);
    float var = fmaxf(fmaf(-mean, mean, t.y * (1.f / 256.f)), 0.f);
    float rstd = __builtin_amdgcn_rsqf(var + 1e-5f);
    const u32x2 ov = {pk_bf16((v4[0] - mean) * rstd, (v4[1] - mean) * rstd), pk_bf16((v4[2] - mean) * rstd, (v4[3] - mean) * rstd)};
    asm volatile("buffer_store_dwordx2 %0, %1, %2, 0 offen" ::"v"(ov), "v"(oo), "s"(rout) : "memory");
  }
}

extern "C" void kernel_launch(void* const* d_in, const int* in_sizes, int n_in, void* d_out, int out_size, void* d_ws, size_t ws_size,
                              hipStream_t stream) {
  Params p;
  p.x = (const u16*)d_in[0]; p.w1a = (const u16*)d_in[1]; p.w1b = (const u16*)d_in[2]; p.wqkv = (const u16*)d_in[3];
  p.wo = (const u16*)d_in[4]; p.wp1 = (const u16*)d_in[5]; p.dw = (const u16*)d_in[6]; p.wp2 = (const u16*)d_in[7];
  p.w2a = (const u16*)d_in[8]; p.w2b = (const u16*)d_in[9];
  p.out = (u16*)d_out; p.ws = (unsigned char*)d_ws;
  const u16* tp[8] = {p.w1a, p.w1b, p.wqkv, p.wo, p.wp1, p.wp2, p.w2a, p.w2b};
  const unsigned rs[8] = {2048, 512, 1536, 512, 1024, 512, 2048, 512}, jm[8] = {512, 131072, 128, 0, 128, 0, 512, 131072};
  const unsigned c2[8] = {128, 128, 512, 128, 512, 128, 128, 128}, cm[8] = {3, 3, 2, 3, 1, 3, 3, 3};
  for (int i = 0; i < 8; ++i) { p.tt[i].ptr = tp[i]; p.tt[i].rs = rs[i]; p.tt[i].jm = jm[i]; p.tt[i].c2 = c2[i]; p.tt[i].cm = cm[i]; p.tt[i].pad0 = 0; p.tt[i].pad1 = 0; }
  (void)hipFuncSetAttribute((const void*)conformer_kernel, hipFuncAttributeMaxDynamicSharedMemorySize, 163840);
  conformer_kernel<<<256, 512, 163840, stream>>>(p);
}
```

```cpp
#include <hip/hip_runtime.h>
#include <hip/hip_bf16.h>
#include <stdint.h>

typedef unsigned short u16;
typedef unsigned long long u64;
typedef float f32x16 __attribute__((ext_vector_type(16)));
typedef float f32x4 __attribute__((ext_vector_type(4)));
typedef float f32x2 __attribute__((ext_vector_type(2)));
typedef __bf16 bf16x8 __attribute__((ext_vector_type(8)));
typedef __bf16 bf16x2 __attribute__((ext_vector_type(2)));
typedef short s16x4 __attribute__((ext_vector_type(4)));
typedef unsigned u32x4 __attribute__((ext_vector_type(4)));
typedef unsigned u32x2 __attribute__((ext_vector_type(2)));
#define LDSP(T, off) ((__attribute__((address_space(3))) T*)(uintptr_t)(off))
#define GAS __attribute__((address_space(1)))
#define DEV __device__ __forceinline__

#define P5SLEEP 6
#define DMAFL ""
#define H1DMA 16
#define APRIO 1

constexpr unsigned OFF_EPOCH = 0;
constexpr unsigned OFF_FLAG = 4096;
constexpr unsigned OFF_KV = 1u << 20;
constexpr int TS = 163840;
constexpr unsigned OFF_P1 = 3u << 20, OFF_ATT = OFF_P1 + 65536, OFF_GLU = OFF_ATT + 16384, OFF_P5 = OFF_GLU + 16384;
constexpr unsigned WS_USED = OFF_P1 + 64u * TS;
constexpr unsigned OFF_PROF = WS_USED;
constexpr unsigned BUFA = 0, BUFB = 16384, RING = 32768;
constexpr unsigned OOB = 0x80000000u;
constexpr unsigned SENT = 0xFFFFFFFFu;
constexpr float LOG2E = 1.4426950408889634f;
constexpr int SPIN_CAP = 1 << 16;

struct Params {
  const u16 *x, *w1a, *w1b, *wqkv, *wo, *wp1, *dw, *wp2, *w2a, *w2b;
  u16* out;
  unsigned char* ws;
  struct { const u16* ptr; unsigned rs, jm, c2, cm, pad0, pad1; } tt[8];
};

DEV unsigned pk_bf16(float a, float b) {
  f32x2 v = {a, b};
  return __builtin_bit_cast(unsigned, __builtin_convertvector(v, bf16x2));
}
DEV float bf_lo(unsigned u) { return __uint_as_float(u << 16); }
DEV float bf_hi(unsigned u) { return __uint_as_float(u & 0xffff0000u); }
DEV float sig2(float y) { return __builtin_amdgcn_rcpf(1.f + __builtin_amdgcn_exp2f(-y)); }
template <int N> DEV void wait_vm() { asm volatile("s_waitcnt vmcnt(%0)" ::"i"(N) : "memory"); }
DEV void bar() { asm volatile("s_waitcnt lgkmcnt(0)\n\ts_barrier" ::: "memory"); }
DEV u32x4 mkrs(const void* base, unsigned bytes) { return (u32x4){(unsigned)(uintptr_t)base, (unsigned)((uintptr_t)base >> 32), bytes, 0x00020000u}; }
template <int SLOT, int STEP> DEV void dma(unsigned voff, const u32x4& rs, unsigned ring) {
  unsigned t;
  asm volatile("s_add_u32 m0, %3, %4\n\ts_mov_b32 %0, %5\n\tbuffer_load_dwordx4 %1, %2, %0 offen" DMAFL " lds" : "=&s"(t) : "v"(voff), "s"(rs), "s"(ring), "i"(SLOT * 1024), "i"(STEP) : "memory", "scc");
}
template <int S0, int S1, int LD> DEV void issue(const u32x4& rs, unsigned voff, unsigned ring) {
  if constexpr (S0 < S1) {
    dma<S0, S0 * 32 * LD>(voff, rs, ring);
    issue<S0 + 1, S1, LD>(rs, voff, ring);
  }
}
template <class T> DEV void PIN(T& x) { asm volatile("" : "+v"(x)); }
#define SB() __builtin_amdgcn_sched_barrier(0)
template <int OFS> DEV void ld128(u32x4& d, unsigned voff, const u32x4& rs) {
  asm volatile("buffer_load_dwordx4 %0, %1, %2, 0 offen offset:%3 sc1" : "=v"(d) : "v"(voff), "s"(rs), "i"(OFS) : "memory");
}
template <int OFS> DEV void ldx(u32x4& d, unsigned voff, const u32x4& rs) {
  asm volatile("buffer_load_dwordx4 %0, %1, %2, 0 offen offset:%3" : "=v"(d) : "v"(voff), "s"(rs), "i"(OFS) : "memory");
}
DEV void ld64(u32x2& d, unsigned voff, const u32x4& rs) {
  asm volatile("buffer_load_dwordx2 %0, %1, %2, 0 offen sc1" : "=v"(d) : "v"(voff), "s"(rs) : "memory");
}
template <int OFS> DEV void st128(const u32x4& d, unsigned voff, const u32x4& rs) {
  asm volatile("buffer_store_dwordx4 %0, %1, %2, 0 offen offset:%3 sc1\n\ts_nop 1" ::"v"(d), "v"(voff), "s"(rs), "i"(OFS) : "memory");
}
DEV void st64(const u32x2& d, unsigned voff, const u32x4& rs) {
  asm volatile("buffer_store_dwordx2 %0, %1, %2, 0 offen sc1" ::"v"(d), "v"(voff), "s"(rs) : "memory");
}
DEV u32x4 clean(const u32x4& v) {
  return (u32x4){min(v.x, SENT - 1), v.y, min(v.z, SENT - 1), v.w};
}
DEV unsigned chk(unsigned m, const u32x4& v) { return max(max(m, v.x), v.z); }
DEV void keep(const u32x4& a) { asm volatile("" ::"v"(a)); }
DEV u32x4 trfrag(unsigned addr, unsigned addr2) {
  s16x4 lo = __builtin_amdgcn_ds_read_tr16_b64_v4i16(LDSP(s16x4, addr));
  s16x4 hi = __builtin_amdgcn_ds_read_tr16_b64_v4i16(LDSP(s16x4, addr2));
  u32x2 a = __builtin_bit_cast(u32x2, lo), b = __builtin_bit_cast(u32x2, hi);
  return (u32x4){a.x, a.y, b.x, b.y};
}
DEV f32x16 mfma(const u32x4& a, const u32x4& b, const f32x16& c) {
  return __builtin_amdgcn_mfma_f32_32x32x16_bf16(__builtin_bit_cast(bf16x8, a), __builtin_bit_cast(bf16x8, b), c, 0, 0, 0);
}
DEV u32x4 packhalf(const f32x16& v, int s) {
  return (u32x4){pk_bf16(v[8 * s], v[8 * s + 1]), pk_bf16(v[8 * s + 2], v[8 * s + 3]), pk_bf16(v[8 * s + 4], v[8 * s + 5]), pk_bf16(v[8 * s + 6], v[8 * s + 7])};
}
DEV float swap32_add(float a, float b) { auto r = __builtin_amdgcn_permlane32_swap(__float_as_uint(a), __float_as_uint(b), false, false); return __uint_as_float(r[0]) + __uint_as_float(r[1]); }
DEV float swap16_add(float a, float b) { auto r = __builtin_amdgcn_permlane16_swap(__float_as_uint(a), __float_as_uint(b), false, false); return __uint_as_float(r[0]) + __uint_as_float(r[1]); }
DEV float xhalf_max(float v) { auto r = __builtin_amdgcn_permlane32_swap(__float_as_uint(v), __float_as_uint(v), false, false); return fmaxf(__uint_as_float(r[0]), __uint_as_float(r[1])); }

DEV void ln_stats(const f32x16& v, unsigned sw) {
  f32x2 s = {v[0], v[1]}, q = s * s;
#pragma unroll
  for (int r = 2; r < 16; r += 2) { f32x2 t = {v[r], v[r + 1]}; s += t; q = __builtin_elementwise_fma(t, t, q); }
  *LDSP(float, sw) = swap32_add(s.x + s.y, q.x + q.y);
}
template <int BUF> DEV void put_act(const f32x16& v, unsigned pa) {
  *LDSP(u32x4, pa + BUF) = packhalf(v, 0);
  *LDSP(u32x4, pa + BUF + 1024) = packhalf(v, 1);
}
template <int DST, int STR> DEV void ln_apply(const f32x16& v, unsigned sr, unsigned pa, float scale) {
  f32x2 st[8];
#pragma unroll
  for (int q = 0; q < 8; ++q) st[q] = *LDSP(f32x2, sr + q * STR);
  f32x2 t = ((st[0] + st[1]) + (st[2] + st[3])) + ((st[4] + st[5]) + (st[6] + st[7]));
  float mean = t.x * (1.f / 256.f);
  float var = fmaxf(fmaf(-mean, mean, t.y * (1.f / 256.f)), 0.f);
  float rstd = __builtin_amdgcn_rsqf(var + 1e-5f) * scale;
  float nb = -mean * rstd;
  f32x16 o;
#pragma unroll
  for (int r = 0; r < 16; ++r) o[r] = fmaf(v[r], rstd, nb);
  put_act<DST>(o, pa);
}
template <int S0, int S1> DEV void preread(u32x4 (&a)[16], unsigned rt) {
#pragma unroll
  for (int s = S0; s < S1; ++s) a[s] = trfrag(rt + s * 1024, rt + 16384 + s * 1024);
  SB();
}
template <int S0, int S1> DEV void preread(u32x4 (&a)[16], unsigned lo, unsigned hi) {
#pragma unroll
  for (int s = S0; s < S1; ++s) a[s] = trfrag(lo + s * 1024, hi + s * 1024);
  SB();
}
template <int S0, int S1> DEV void pin(u32x4 (&a)[16]) {
#pragma unroll
  for (int s = S0; s < S1; ++s) asm volatile("" : "+v"(a[s]));
}
template <int N> struct IC { static constexpr int v = N; };
template <int I, int N, class F> DEV void sfor(F&& f) { if constexpr (I < N) { f(IC<I>{}); sfor<I + 1, N>(f); } }
template <bool SWAP, bool TRB, class F> DEV f32x16 gemm(u32x4 (&a)[16], unsigned bb, f32x16 acc, F&& hook) {
  u32x4 b[16];
  constexpr int AH = TRB ? 3 : 4;
#pragma unroll
  for (int s = 0; s < AH; ++s) b[s] = TRB ? trfrag(bb + (s << 10), bb + (s << 10) + 512) : *LDSP(u32x4, bb + (s << 10));
  SB();
  sfor<0, 16>([&](auto I) {
    constexpr int s = I.v;
    hook(I);
    if constexpr (s + AH < 16) b[s + AH] = TRB ? trfrag(bb + ((s + AH) << 10), bb + ((s + AH) << 10) + 512) : *LDSP(u32x4, bb + ((s + AH) << 10));
    SB();
    acc = SWAP ? mfma(b[s], a[s], acc) : mfma(a[s], b[s], acc);
    SB();
  });
  return acc;
}
DEV void wait_lds() { asm volatile("s_waitcnt lgkmcnt(0)" ::: "memory"); }
DEV void bar_only() { asm volatile("s_barrier" ::: "memory"); }
DEV void poll(unsigned voff, const u32x2& tag, const u32x4& rs) {
#pragma unroll 1
  for (int spins = 0; spins < SPIN_CAP; ++spins) {
    u32x2 v; ld64(v, voff, rs);
    asm volatile("s_waitcnt vmcnt(0)" : "+v"(v)::"memory");
    if (__all(v.x == tag.x && v.y == tag.y)) break;
    __builtin_amdgcn_s_sleep(1);
  }
}
template <int R0, int R1> DEV void silu2(f32x16& v) {
#pragma unroll
  for (int r = R0; r < R1; ++r) v[r] *= sig2(v[r]);
  asm volatile("" : "+v"(v));
}
#define STAMP(i)
#define FS(i)

__global__ void __launch_bounds__(512, 2) conformer_kernel(Params p_) {
  STAMP(0);
  const int wg = blockIdx.x;
  const int j = wg & 3, tile = ((wg >> 2) & 1) * 32 + (wg >> 3), cu = wg >> 3;
  const int b = tile >> 5, ti = tile & 31;
  const int w = __builtin_amdgcn_readfirstlane(threadIdx.x >> 6);
  const __attribute__((address_space(4))) Params* kp = (const __attribute__((address_space(4))) Params*)__builtin_amdgcn_kernarg_segment_ptr();
  const u16* tptr = kp->tt[w].ptr;
  unsigned trs = kp->tt[w].rs, tjm = kp->tt[w].jm, tc2 = kp->tt[w].c2, tcm = kp->tt[w].cm;
  struct { const u16 *x, *w1a, *w1b, *wqkv, *wo, *wp1, *dw, *wp2, *w2a, *w2b; u16* out; unsigned char* ws; } p =
      {kp->x, kp->w1a, kp->w1b, kp->wqkv, kp->wo, kp->wp1, kp->dw, kp->wp2, kp->w2a, kp->w2b, kp->out, kp->ws};
  asm volatile("" : "+s"(tptr), "+s"(trs), "+s"(tjm), "+s"(tc2), "+s"(tcm), "+s"(p.x), "+s"(p.w1a), "+s"(p.w1b), "+s"(p.wqkv), "+s"(p.wo),
               "+s"(p.wp1), "+s"(p.dw), "+s"(p.wp2), "+s"(p.w2a), "+s"(p.w2b), "+s"(p.out), "+s"(p.ws));
  FS(13);
  const int nvalid = ti == 31 ? 8 : 32;
  const unsigned ring = RING + w * 16384;
  const u32x4 rws = mkrs(p.ws, WS_USED);
  int lane, h, tok;
  unsigned trofs, rt, crow, ccol, v1024, v256, l16, pa, sa, sw, sr, oGLUst;
  auto lane0 = [&]() {
    asm volatile("v_mbcnt_lo_u32_b32 %0, -1, 0\n\tv_mbcnt_hi_u32_b32 %0, -1, %0" : "=v"(lane));
    h = lane >> 5; tok = lane & 31;
  };
  auto lanevals = [&]() {
    trofs = (h * 4 + ((lane & 15) >> 2)) * 64 + ((lane >> 4) & 1) * 32 + (lane & 3) * 8;
    rt = RING + (w >> 1) * 32768 + (h * 4 + ((lane & 15) >> 2)) * 128 + (((w ^ (lane >> 3)) & 1) * 64) + ((lane >> 4) & 1) * 32 + (lane & 3) * 8;
    crow = (lane >> 3) + 8 * (w & 1);
    ccol = ((lane & 7) ^ ((lane >> 2) & 4)) * 16;
    v1024 = crow * 2048 + 512 * j + (w >> 1) * 128 + ccol; v256 = crow * 512 + (w >> 1) * 128 + ccol;
    l16 = lane * 16; pa = l16 + w * 2048; sr = tok * 8; sa = sr + h * 4; sw = sa + w * 256;
    oGLUst = OFF_GLU + (unsigned)(tile * TS + ((lane >> 4) & 1) * 8192 + (2 * j + (w >> 1)) * 1024 + (lane & 1) * 512 + ((w & 1) * 8 + ((lane & 15) >> 1)) * 32 + 16 * h);
    PIN(rt); PIN(v1024); PIN(v256); PIN(l16); PIN(pa); PIN(sw); PIN(sr); PIN(oGLUst);
  };
  lane0();
  const u32x4 sent = {SENT, SENT, SENT, SENT};
  auto flagoff = [&](int hop, int t, int jj, int wave) { return OFF_FLAG + (unsigned)((((hop * 64 + t) * 4 + jj) * 8 + wave) * 128); };
  const u32x4 r1a = mkrs(p.w1a, 524288), r1b = mkrs(p.w1b + 65536 * j, 131072);
  const f32x16 zero16 = {0};
  auto nohook = [&](auto) {};
  auto touch = [&]() { return (8 * cu + (lane >> 2)) * trs + j * tjm + min((unsigned)lane & 3u, tcm) * tc2; };
  f32x16 xres;
  u32x2 tag;
  u32x4 wr[16], wr2[16];
  unsigned oP1;
  {
    const u32x4 rx = mkrs(p.x, 2000u * 512u);
    const unsigned xo = tok < nvalid ? (unsigned)((b * 1000 + ti * 32 + tok) * 512 + (32 * w + 16 * h) * 2) : OOB;
    u32x4 x0, x1;
    ldx<0>(x0, xo, rx); ldx<16>(x1, xo, rx);
    unsigned junk = 0;
    if (lane < 32 && w < 4) asm volatile("global_load_dword %0, %1, %2" : "=v"(junk) : "v"(touch()), "s"(tptr) : "memory");
    ld64(tag, OFF_EPOCH + b * 256, rws);
    SB();
    FS(14);
    lanevals();
    oP1 = OFF_P1 + (unsigned)(tile * TS) + pa;
    st128<0>(sent, oP1 + j * 16384, rws); st128<1024>(sent, oP1 + j * 16384, rws);
    wait_vm<3>();
    asm volatile("" : "+v"(x0), "+v"(x1), "+v"(junk)::"memory");
    FS(15);
    bar_only();
    issue<0, 4, 1024>(r1a, v1024, ring);
    SB();
    auto a0 = __builtin_amdgcn_permlane32_swap(x0.x, x0.z, false, false), a1 = __builtin_amdgcn_permlane32_swap(x0.y, x0.w, false, false);
    auto c0 = __builtin_amdgcn_permlane32_swap(x1.x, x1.z, false, false), c1 = __builtin_amdgcn_permlane32_swap(x1.y, x1.w, false, false);
    const unsigned xv[8] = {a0[0], a1[0], c0[0], c1[0], a0[1], a1[1], c0[1], c1[1]};
#pragma unroll
    for (int r = 0; r < 8; ++r) { xres[2 * r] = bf_lo(xv[r]); xres[2 * r + 1] = bf_hi(xv[r]); }
    STAMP(1);
    ln_stats(xres, BUFB + sw);
    issue<4, 8, 1024>(r1a, v1024, ring);
    bar();
    ln_apply<BUFA, 256>(xres, BUFB + sr, pa, LOG2E);
    SB();
    issue<8, 16, 1024>(r1a, v1024, ring);
    asm volatile("s_waitcnt vmcnt(8) lgkmcnt(0)" : "+v"(tag)::"memory");
    bar_only();
    preread<0, 4>(wr, rt);
    u64 e = (((u64)tag.y << 32) | tag.x) + 1;
    tag = (u32x2){(unsigned)e, (unsigned)(e >> 32)};
    if (w < 4) __builtin_amdgcn_s_setprio(2);
  }
  STAMP(2);
  {
    f32x16 acc = gemm<false, false>(wr, BUFA + l16, zero16, [&](auto I) {
      constexpr int s = I.v;
      if constexpr (s == 0) preread<4, 8>(wr, rt);
      if constexpr (s == 4) { pin<4, 8>(wr); wait_vm<0>(); bar_only(); if (w == 7) st64(tag, lane == 0 ? flagoff(0, tile, j, 0) : OOB, rws); issue<0, 2, 256>(r1b, v256, ring); preread<8, 12>(wr, rt); }
      if constexpr (s == 8) preread<12, 16>(wr, rt);
      if constexpr ((s & 1) && s > 4) issue<s / 2, s / 2 + 1, 256>(r1b, v256, ring);
      if constexpr (s == 15) { pin<15, 16>(wr); bar_only(); }
    });
    FS(10);
    sfor<0, 8>([&](auto I) { issue<8 + I.v, 9 + I.v, 256>(r1b, v256, ring); silu2<2 * I.v, 2 * I.v + 2>(acc); SB(); });
    FS(11);
    put_act<BUFB>(acc, pa);
    asm volatile("s_waitcnt vmcnt(8) lgkmcnt(0)" ::: "memory");
    FS(12);
    if (w < 4) { st128<0>(sent, OFF_ATT + (unsigned)(tile * TS + j * 4096 + w * 1024) + l16, rws); st128<0>(sent, oGLUst, rws); }
  }
  bar_only();
  preread<0, 4>(wr, rt);
  STAMP(3);
  const u32x4 rq = mkrs(p.wqkv, 393216), ro = mkrs(p.wo, 131072);
  {
    u32x2 fl;
    unsigned fo = flagoff(0, tile, (j + 1 + (lane & 1) + ((lane >> 1) & 1)) & 3, 0);
    f32x16 acc = gemm<false, false>(wr, BUFB + l16, zero16, [&](auto I) {
      constexpr int s = I.v;
      if constexpr (s == 0) preread<4, 8>(wr, rt);
      if constexpr (s == 5) { wait_vm<0>(); bar_only(); preread<8, 12>(wr, rt); }
      if constexpr (s == 8) preread<12, 16>(wr, rt);
      if constexpr (s == 6) ld64(fl, fo, rws);
    });
#pragma unroll
    for (int r = 0; r < 16; ++r) acc[r] *= 0.5f / LOG2E;
    st128<0>(clean(packhalf(acc, 0)), oP1 + j * 16384, rws);
    st128<1024>(clean(packhalf(acc, 1)), oP1 + j * 16384, rws);
    STAMP(4);
    unsigned v768 = crow * 1536 + (w < 4 ? 512 + (w >> 1) * 512 : 0) + 128 * j + ccol;
    PIN(v768);
    bar_only();
    if (w < 6) { issue<0, H1DMA, 768>(rq, v768, ring); wait_vm<H1DMA + 2>(); } else wait_vm<2>();
    PIN(fl);
    STAMP(5);
    xres += acc;
    PIN(xres);
    u32x4 idf[2];
    {
      const int n = ((tok >> 2) & 1) == h ? 2 * (tok >> 3) + ((tok >> 1) & 1) : 8;
      const unsigned one = 0x3F80u << (16 * (tok & 1));
#pragma unroll
      for (int q = 0; q < 8; ++q) idf[q >> 2][q & 3] = n == q ? one : 0u;
      PIN(idf[0]); PIN(idf[1]);
    }
    unsigned o0 = oP1 + ((j + 1) & 3) * 16384, o1 = oP1 + ((j + 2) & 3) * 16384, o2 = oP1 + ((j + 3) & 3) * 16384;
    PIN(o0); PIN(o1); PIN(o2);
    bar_only();
    if (!__all(fl.x == tag.x && fl.y == tag.y)) poll(fo, tag, rws);
    u32x4 pv[6];
#pragma unroll 1
    for (int spins = 0; spins < SPIN_CAP; ++spins) {
      ld128<0>(pv[0], o0, rws); ld128<1024>(pv[1], o0, rws); ld128<0>(pv[2], o1, rws); ld128<1024>(pv[3], o1, rws); ld128<0>(pv[4], o2, rws); ld128<1024>(pv[5], o2, rws);
      asm volatile("s_waitcnt vmcnt(0)" : "+v"(pv[0]), "+v"(pv[1]), "+v"(pv[2]), "+v"(pv[3]), "+v"(pv[4]), "+v"(pv[5])::"memory");
      if (__all(chk(chk(chk(chk(chk(chk(0, pv[0]), pv[1]), pv[2]), pv[3]), pv[4]), pv[5]) != SENT)) break;
    }
    STAMP(6);
#pragma unroll
    for (int q = 0; q < 6; ++q) xres = mfma(idf[q & 1], pv[q], xres);
    SB();
    STAMP(7);
  }
  ln_stats(xres, BUFA + sw);
  bar();
  ln_apply<BUFB, 256>(xres, BUFA + sr, pa, 1.f);
  wait_lds();
  if (w < 6) preread<0, 6>(wr, rt);
  bar_only();
  STAMP(8);
  unsigned oKV = OFF_KV + (unsigned)((b * 4 + j) * 32 * 8192) + l16;
  if (w < 4) {
    f32x16 acc;
    unsigned o = oKV + ti * 8192 + w * 2048;
    auto hk = [&](auto I) { if constexpr (I.v == 1) PIN(o); if constexpr (I.v < 10) preread<I.v + 6, I.v + 7>(wr, rt); };
    if (w >= 2) acc = gemm<true, false>(wr, BUFB + l16, zero16, hk); else acc = gemm<false, false>(wr, BUFB + l16, zero16, hk);
    st128<0>(packhalf(acc, 0), o, rws);
    st128<1024>(packhalf(acc, 1), o, rws);
    wait_vm<0>();
  } else if (w < 6) {
    f32x16 acc = gemm<false, false>(wr, BUFB + l16, zero16, [&](auto I) { if constexpr (I.v < 10) preread<I.v + 6, I.v + 7>(wr, rt); });
#pragma unroll
    for (int r = 0; r < 16; ++r) acc[r] *= 0.125f * LOG2E;
    put_act<BUFA>(acc, pa - 8192);
  } else {
    issue<0, 15, 256>(ro, v256, ring);
  }
  unsigned fo2 = lane == 0 ? flagoff(1, tile, j, 0) : OOB, to2 = OFF_KV + (unsigned)(((b * 4 + j) * 32 + ti) * 8192 + (w & 1) * 4096 + lane * 64);
  PIN(fo2); PIN(to2);
  STAMP(9);
  bar();
  unsigned junk3 = 0;
  if (w == 7) st64(tag, fo2, rws);
  if (w >= 6) asm volatile("buffer_load_dword %0, %1, %2, 0 offen sc1" : "=v"(junk3) : "v"(to2), "s"(rws) : "memory");
  unsigned junk4 = 0, junk5 = 0;
  if (lane < 32 && w >= 4) asm volatile("global_load_dword %0, %1, %2" : "=v"(junk4) : "v"(touch()), "s"(tptr) : "memory");
  if (lane < 4 && w == 5) asm volatile("global_load_dword %0, %1, %2" : "=v"(junk5) : "v"((unsigned)(min(cu, 30) * 512 + lane * 128)), "s"(p.dw) : "memory");
  u32x4 qf[4];
#pragma unroll
  for (int s = 0; s < 4; ++s) qf[s] = *LDSP(u32x4, BUFA + s * 1024 + l16);
  if (w < 6) issue<0, 15, 256>(ro, v256, ring); else __builtin_amdgcn_s_sleep(12);
  asm volatile("" : "+v"(qf[0]), "+v"(qf[1]), "+v"(qf[2]), "+v"(qf[3]));
  oKV += 4 * w * 8192;
  PIN(oKV);
  bar();
  poll(flagoff(1, b * 32 + 4 * w + (lane & 3), j, 0), tag, rws);
  PIN(junk3); PIN(junk4); PIN(junk5);
  STAMP(10);
  unsigned oP5st;
  {
    const __amdgpu_buffer_rsrc_t rkv = __builtin_amdgcn_make_buffer_rsrc(p.ws, 0, WS_USED, 0x00020000);
    u32x4 kf[4][4], vf[4][4];
#pragma unroll
    for (int t = 0; t < 4; ++t)
#pragma unroll
      for (int s = 0; s < 4; ++s) kf[t][s] = __builtin_amdgcn_raw_buffer_load_b128(rkv, oKV + s * 1024, t * 8192, 16);
    SB();
    f32x16 sc[4];
    float mx = -INFINITY;
#pragma unroll
    for (int t = 0; t < 4; ++t) {
      f32x16 a = {0};
#pragma unroll
      for (int s = 0; s < 4; ++s) a = mfma(kf[t][s], qf[s], a);
      SB();
#pragma unroll
      for (int s = 0; s < 4; ++s) vf[t][s] = __builtin_amdgcn_raw_buffer_load_b128(rkv, oKV + s * 1024, t * 8192 + 4096, 16);
      SB();
      if (t == 3 && w == 7) {
#pragma unroll
        for (int r = 4; r < 16; ++r) a[r] = -INFINITY;
      }
      sc[t] = a;
      if (t > 0) {
#pragma unroll
        for (int r = 0; r < 16; r += 2) mx = fmaxf(fmaxf(mx, sc[t - 1][r]), sc[t - 1][r + 1]);
        SB();
      }
    }
#pragma unroll
    for (int r = 0; r < 16; r += 2) mx = fmaxf(fmaxf(mx, sc[3][r]), sc[3][r + 1]);
    mx = xhalf_max(mx);
    const f32x2 mx2 = {mx, mx};
    f32x2 s2 = {0.f, 0.f};
    f32x16 o0 = {0}, o1 = {0};
#pragma unroll
    for (int t = 0; t < 4; ++t) {
      unsigned pk[8];
#pragma unroll
      for (int r = 0; r < 8; ++r) {
        f32x2 d = (f32x2){sc[t][2 * r], sc[t][2 * r + 1]} - mx2;
        d = (f32x2){__builtin_amdgcn_exp2f(d.x), __builtin_amdgcn_exp2f(d.y)};
        s2 += d;
        pk[r] = pk_bf16(d.x, d.y);
      }
      const u32x4 p0 = {pk[0], pk[1], pk[2], pk[3]}, p1 = {pk[4], pk[5], pk[6], pk[7]};
      o0 = mfma(vf[t][0], p0, o0); o1 = mfma(vf[t][2], p0, o1);
      o0 = mfma(vf[t][1], p1, o0); o1 = mfma(vf[t][3], p1, o1);
      SB();
    }
    lane0(); lanevals();
    float sum = s2.x + s2.y;
    sum = swap32_add(sum, sum);
    *LDSP(float, ring + 15 * 1024 + sa) = h ? sum : mx;
    SB();
    put_act<BUFA>(o0, pa + w * 2048);
    put_act<BUFA + 2048>(o1, pa + w * 2048);
  }
  STAMP(11);
  bar();
  {
    f32x2 L[8];
    u32x2 pv[8];
#pragma unroll
    for (int q = 0; q < 8; ++q) L[q] = *LDSP(f32x2, RING + q * 16384 + 15 * 1024 + (lane >> 1) * 8);
#pragma unroll
    for (int q = 0; q < 8; ++q) pv[q] = *LDSP(u32x2, BUFA + q * 4096 + w * 512 + lane * 8);
    SB();
    float lm = fmaxf(fmaxf(fmaxf(L[0].x, L[1].x), fmaxf(L[2].x, L[3].x)), fmaxf(fmaxf(L[4].x, L[5].x), fmaxf(L[6].x, L[7].x)));
    float om = 0.f;
    f32x2 a0 = {0, 0}, a1 = {0, 0};
#pragma unroll
    for (int q = 0; q < 8; ++q) {
      float f = __builtin_amdgcn_exp2f(L[q].x - lm);
      om = fmaf(f, L[q].y, om);
      const f32x2 f2 = {f, f};
      a0 = __builtin_elementwise_fma(f2, (f32x2){bf_lo(pv[q].x), bf_hi(pv[q].x)}, a0);
      a1 = __builtin_elementwise_fma(f2, (f32x2){bf_lo(pv[q].y), bf_hi(pv[q].y)}, a1);
    }
    om = __builtin_amdgcn_rcpf(om);
    a0 *= om; a1 *= om;
    st64((u32x2){min(pk_bf16(a0.x, a0.y), SENT - 1), pk_bf16(a1.x, a1.y)}, OFF_ATT + (unsigned)(tile * TS + j * 4096 + w * 512 + lane * 8), rws);
  }
  preread<0, 15>(wr, rt);
  oP5st = ((tok >> 3) == j) ? OOB : OFF_P5 + (unsigned)(tile * TS + ((tok >> 3) * 8 + w) * 2048 + j * 256 + h * 128 + (tok & 7) * 16);
  st128<0>(sent, oP5st, rws); st128<1024>(sent, oP5st, rws);
  bar();
  STAMP(12);
  issue<15, 16, 256>(ro, v256, ring);
  if (ti == 0 && j == 0 && w == 4) st64(tag, lane == 0 ? OFF_EPOCH + b * 256 : OOB, rws);
  const u32x4 rp1 = mkrs(p.wp1, 262144), rp2 = mkrs(p.wp2, 131072), rdw = mkrs(p.dw, 31 * 512);
  const u32x4 r2a = mkrs(p.w2a, 524288), r2b = mkrs(p.w2b + 65536 * j, 131072);
  const unsigned v512 = ((lane >> 2) * 512 + 64 * j + 16 * w + (lane & 1) * 8 + ((lane >> 1) & 1) * 256) * 2;
  pin<0, 15>(wr);
  { const unsigned vd = (lane >> 2) * 512 + (lane & 3) * 16 + 64 * w; dma<0, 0>(vd, rdw, BUFB + w * 2048); dma<1, 8192>(vd, rdw, BUFB + w * 2048); }
  if (w < 4) issue<0, 4, 512>(rp1, v512, ring); else issue<0, 4, 1024>(r2a, v1024, ring);
  unsigned z31 = BUFB + w * 2048 + 31 * 64 + (lane & 15) * 4, sw3 = ring + 3 * 1024 + sa, sr3 = RING + 3 * 1024 + sr;
  unsigned r1lo = w < 4 ? ring + trofs : rt, r1hi = r1lo + (w < 4 ? 512 : 16384);
  PIN(r1lo); PIN(r1hi); PIN(trofs); PIN(z31); PIN(sw3); PIN(sr3);
  {
    const unsigned o = OFF_ATT + (unsigned)(tile * TS) + pa;
    u32x4 a0, a1;
    ld128<0>(a0, o, rws); ld128<1024>(a1, o, rws);
    bar_only();
    if (w < 4) issue<4, 12, 512>(rp1, v512, ring); else issue<4, 12, 1024>(r2a, v1024, ring);
    asm volatile("s_waitcnt vmcnt(8)" : "+v"(a0), "+v"(a1)::"memory");
    *LDSP(unsigned, z31) = 0;
#pragma unroll 1
    for (int spins = 0; spins < SPIN_CAP && !__all(chk(chk(0, a0), a1) != SENT); ++spins) {
      ld128<0>(a0, o, rws); ld128<1024>(a1, o, rws);
      asm volatile("s_waitcnt vmcnt(0)" : "+v"(a0), "+v"(a1)::"memory");
    }
    *LDSP(u32x4, BUFA + pa) = a0;
    *LDSP(u32x4, BUFA + pa + 1024) = a1;
  }
  bar();
  STAMP(13);
  xres = gemm<false, false>(wr, BUFA + l16, xres, [&](auto I) {
    constexpr int s = I.v;
    if constexpr (s == 1) { preread<15, 16>(wr, rt); preread<0, 4>(wr2, r1lo, r1hi); }
    if constexpr (s == 1 || s == 3 || s == 5) { if (w < 4) issue<12 + s / 2, 13 + s / 2, 512>(rp1, v512, ring); else issue<12 + s / 2, 13 + s / 2, 1024>(r2a, v1024, ring); }
    if constexpr (s == 8) { pin<15, 16>(wr); pin<0, 4>(wr2); wait_vm<3>(); bar_only(); preread<4, 8>(wr2, r1lo, r1hi); }
    if constexpr (s == 9) { if (w < 4) issue<15, 16, 512>(rp1, v512, ring); else issue<15, 16, 1024>(r2a, v1024, ring); }
    if constexpr (s == 12) preread<8, 12>(wr2, r1lo, r1hi);
    if constexpr (s == 11 || s == 13 || s == 15) issue<s / 2 - 5, s / 2 - 4, 256>(rp2, v256, ring);
  });
  ln_stats(xres, sw3);
  wait_vm<3>();
  bar();
  pin<4, 12>(wr2);
  if (w < 4) issue<4, 8, 256>(rp2, v256, ring);
  ln_apply<BUFA, 16384>(xres, sr3, pa, 1.f);
  SB();
  preread<12, 16>(wr2, r1lo, r1hi);
  if (w < 4) issue<8, 12, 256>(rp2, v256, ring);
  bar();
  STAMP(14);
  pin<12, 16>(wr2);
  u32x2 tw[2][9];
  auto build_tw = [&]() {
  {
    const int c = lane >> 2, i = lane & 3;
    const unsigned tc = BUFB + w * 2048 + c * 4, tb = tc - (i + 1) * 64;
    unsigned t[36];
#pragma unroll
    for (int n = 0; n < 36; ++n) {
      if (n == 0 || n == 35) t[n] = 0;
      else if (n < 4 || n > 32) t[n] = *LDSP(unsigned, tc + min((unsigned)(n - 1 - i), 31u) * 64);
      else t[n] = *LDSP(unsigned, tb + n * 64);
    }
#pragma unroll
    for (int u = 0; u < 9; ++u) {
      tw[0][u] = (u32x2){__builtin_amdgcn_perm(t[4 * u + 1], t[4 * u], 0x05040100u), __builtin_amdgcn_perm(t[4 * u + 3], t[4 * u + 2], 0x05040100u)};
      tw[1][u] = (u32x2){__builtin_amdgcn_perm(t[4 * u + 1], t[4 * u], 0x07060302u), __builtin_amdgcn_perm(t[4 * u + 3], t[4 * u + 2], 0x07060302u)};
      PIN(tw[0][u]); PIN(tw[1][u]);
    }
  }
  };
  if (w < 4) {
    f32x16 acc = gemm<true, false>(wr2, BUFA + l16, zero16, [&](auto I) {
      if constexpr (I.v == 1) { issue<3, 4, 256>(rp2, v256, ring); issue<12, 16, 256>(rp2, v256, ring); }
    });
    float g[8];
    const int e = (lane >> 4) & 1;
#pragma unroll
    for (int r = 0; r < 8; ++r) {
      auto sw = __builtin_amdgcn_permlane16_swap(__float_as_uint(acc[r]), __float_as_uint(acc[r + 8]), false, false);
      g[r] = __uint_as_float(sw[0]) * sig2(LOG2E * __uint_as_float(sw[1]));
    }
    if (ti == 31) {
#pragma unroll
      for (int r = 0; r < 8; ++r) g[r] = ((r & 3) + 8 * (r >> 2) + 16 * e + 4 * h < 8) ? g[r] : 0.f;
    }
    auto sx = __builtin_amdgcn_permlane32_swap(pk_bf16(g[0], g[1]), pk_bf16(g[4], g[5]), false, false);
    auto sy = __builtin_amdgcn_permlane32_swap(pk_bf16(g[2], g[3]), pk_bf16(g[6], g[7]), false, false);
    st128<0>(clean((u32x4){sx[0], sy[0], sx[1], sy[1]}), oGLUst, rws);
    wait_vm<1>();
  } else {
    issue<3, 16, 256>(rp2, v256, ring);
    wait_vm<0>();
  }
  STAMP(15);
  bar_only();
  preread<0, 16>(wr, rt);
  bar();
  pin<0, 16>(wr);
  if (w < 4) issue<0, 6, 1024>(r2a, v1024, ring); else issue<0, 4, 256>(r2b, v256, ring);
  build_tw();
  {
    const int c = lane >> 2, jj = lane & 3;
    unsigned o0 = OFF_GLU + (unsigned)(tile * TS + (jj < 2 ? 8192 - TS : 0) + w * 1024 + c * 32 + (jj & 1) * 16), o1 = o0 + TS;
    if (jj < 2 && ti == 0) o0 = OOB;
    if (jj >= 2 && ti == 31) o1 = OOB;
    unsigned pc = BUFB + w * 2048 + c * 128 + jj * 16;
    PIN(o0); PIN(o1); PIN(pc);
    u32x4 c0[2], c1[2];
#pragma unroll 1
    for (int spins = 0; spins < SPIN_CAP; ++spins) {
      ld128<0>(c0[0], o0, rws); ld128<0>(c1[0], o1, rws); ld128<512>(c0[1], o0, rws); ld128<512>(c1[1], o1, rws);
      asm volatile("s_waitcnt vmcnt(0)" : "+v"(c0[0]), "+v"(c1[0]), "+v"(c0[1]), "+v"(c1[1])::"memory");
      if (__all(chk(chk(chk(chk(0, c0[0]), c1[0]), c0[1]), c1[1]) != SENT)) break;
    }
    STAMP(16);
    if (w < 4) issue<6, 11, 1024>(r2a, v1024, ring);
    f32x4 d[2][2];
#pragma unroll
    for (int g = 0; g < 2; ++g) {
      u32x4 ch[5];
      ch[0] = c0[g]; ch[4] = c1[g];
#pragma unroll
      for (int dd = 0; dd < 4; ++dd) {
        ch[1][dd] = __builtin_amdgcn_mov_dpp(jj < 1 ? c1[g][dd] : c0[g][dd], 0x39, 0xf, 0xf, false);
        ch[2][dd] = __builtin_amdgcn_mov_dpp(jj < 2 ? c1[g][dd] : c0[g][dd], 0x4e, 0xf, 0xf, false);
        ch[3][dd] = __builtin_amdgcn_mov_dpp(jj < 3 ? c1[g][dd] : c0[g][dd], 0x93, 0xf, 0xf, false);
      }
      f32x4 d0 = {0, 0, 0, 0}, d1 = {0, 0, 0, 0};
#pragma unroll
      for (int u = 0; u < 9; ++u) {
        u32x2 q0 = {ch[u >> 1][2 * (u & 1)], ch[u >> 1][2 * (u & 1) + 1]};
        u32x2 q1 = {ch[(u + 1) >> 1][2 * ((u + 1) & 1)], ch[(u + 1) >> 1][2 * ((u + 1) & 1) + 1]};
        d0 = __builtin_amdgcn_mfma_f32_4x4x4bf16_1k(__builtin_bit_cast(s16x4, tw[g][u]), __builtin_bit_cast(s16x4, q0), d0, 0, 0, 0);
        d1 = __builtin_amdgcn_mfma_f32_4x4x4bf16_1k(__builtin_bit_cast(s16x4, tw[g][u]), __builtin_bit_cast(s16x4, q1), d1, 0, 0, 0);
      }
      d[g][0] = d0; d[g][1] = d1;
      if (g == 0 && w < 4) issue<11, 16, 1024>(r2a, v1024, ring);
    }
#pragma unroll
    for (int g = 0; g < 2; ++g) {
#pragma unroll
      for (int r = 0; r < 4; ++r) { d[g][0][r] *= sig2(LOG2E * d[g][0][r]); d[g][1][r] *= sig2(LOG2E * d[g][1][r]); }
      *LDSP(u32x4, pc + g * 64) = (u32x4){pk_bf16(d[g][0][0], d[g][0][1]), pk_bf16(d[g][0][2], d[g][0][3]), pk_bf16(d[g][1][0], d[g][1][1]), pk_bf16(d[g][1][2], d[g][1][3])};
    }
    if (w < 4) wait_vm<0>(); else issue<4, 8, 256>(r2b, v256, ring);
  }
  bar();
  STAMP(17);
  xres = gemm<false, true>(wr, trofs + BUFB, xres, [&](auto I) {
    constexpr int s = I.v;
    if constexpr (s & 1) { if (w < 4) preread<s - 1, s + 1>(wr2, rt); else issue<8 + s / 2, 9 + s / 2, 256>(r2b, v256, ring); }
  });
  ln_stats(xres, BUFA + sw);
  bar();
  pin<0, 16>(wr2);
  if (w < 4) issue<0, 6, 256>(r2b, v256, ring);
  ln_apply<BUFB, 256>(xres, BUFA + sr, pa, LOG2E);
  SB();
  if (w < 4) issue<6, 12, 256>(r2b, v256, ring);
  bar();
  STAMP(18);
  {
    f32x16 acc = gemm<false, false>(wr2, BUFB + l16, zero16, [&](auto I) {
      constexpr int s = I.v;
      if constexpr (s == 1) { if (w < 4) issue<12, 16, 256>(r2b, v256, ring); }
    });
    silu2<0, 8>(acc);
    wait_vm<0>(); bar_only();
    preread<0, 8>(wr, rt);
    silu2<8, 16>(acc);
    put_act<BUFA>(acc, pa);
  }
  bar();
  STAMP(19);
  {
    unsigned o = ((lane >> 4) == j) ? OOB : OFF_P5 + (unsigned)(tile * TS + j * 16384) + pa;
    unsigned oa = ring + h * 128 + (tok & 7) * 16;
    f32x16 acc = gemm<false, false>(wr, BUFA + l16, zero16, [&](auto I) {
      if constexpr (I.v == 1) { PIN(o); PIN(oa); }
      if constexpr (I.v < 8) preread<I.v + 8, I.v + 9>(wr, rt);
    });
#pragma unroll
    for (int r = 0; r < 16; ++r) acc[r] *= 0.5f / LOG2E;
    st128<0>(clean(packhalf(acc, 0)), oP5st, rws);
    st128<1024>(clean(packhalf(acc, 1)), oP5st, rws);
    STAMP(20);
    xres += acc;
    if ((tok >> 3) == j) {
#pragma unroll
      for (int g = 0; g < 4; ++g)
        *LDSP(f32x4, oa + ((g >> 1) + 2 * (g & 1)) * 256) = (f32x4){xres[4 * g], xres[4 * g + 1], xres[4 * g + 2], xres[4 * g + 3]};
    }
    wait_lds();
    f32x4 own = *LDSP(f32x4, ring + l16);
    const int otok = 8 * j + (lane & 7), qg = lane >> 4;
    unsigned oo = otok < nvalid ? (unsigned)(((b * 1000 + ti * 32 + otok) * 256 + 32 * w + 8 * ((qg >> 1) + 2 * (qg & 1)) + 4 * ((lane >> 3) & 1)) * 2) : OOB;
    unsigned sf = BUFB + (lane & 7) * 8, sg = (lane & 24) == 0 ? sf + w * 64 + h * 4 : BUFB + 1024;
    const u32x4 rout = mkrs(p.out, 2000u * 512u);
    PIN(own); PIN(oo); PIN(sf); PIN(sg);
    __builtin_amdgcn_s_sleep(P5SLEEP);
    u32x4 a0, a1;
#pragma unroll 1
    for (int spins = 0; spins < SPIN_CAP; ++spins) {
      ld128<0>(a0, o, rws); ld128<1024>(a1, o, rws);
      asm volatile("s_waitcnt vmcnt(0)" : "+v"(a0), "+v"(a1)::"memory");
      if (__all(chk(chk(0, a0), a1) != SENT)) break;
    }
    STAMP(21);
    float t8[8], v4[4];
#pragma unroll
    for (int r = 0; r < 2; ++r) {
      auto s0 = __builtin_amdgcn_permlane32_swap(a0[r], a0[r + 2], false, false), s1 = __builtin_amdgcn_permlane32_swap(a1[r], a1[r + 2], false, false);
      t8[2 * r] = bf_lo(s0[0]) + bf_lo(s0[1]); t8[2 * r + 1] = bf_hi(s0[0]) + bf_hi(s0[1]);
      t8[4 + 2 * r] = bf_lo(s1[0]) + bf_lo(s1[1]); t8[4 + 2 * r + 1] = bf_hi(s1[0]) + bf_hi(s1[1]);
    }
#pragma unroll
    for (int r = 0; r < 4; ++r) v4[r] = swap16_add(t8[r], t8[4 + r]) + own[r];
    float s = (v4[0] + v4[1]) + (v4[2] + v4[3]);
    float q = fmaf(v4[0], v4[0], v4[1] * v4[1]) + fmaf(v4[2], v4[2], v4[3] * v4[3]);
    float z = swap32_add(s, q);
    z = swap16_add(z, z);
    z += __uint_as_float(__builtin_amdgcn_mov_dpp(__float_as_uint(z), 0x128, 0xf, 0xf, false));
    *LDSP(float, sg) = z;
    bar();
    f32x2 st[8];
#pragma unroll
    for (int pp = 0; pp < 8; ++pp) st[pp] = *LDSP(f32x2, sf + pp * 64);
    f32x2 t = ((st[0] + st[1]) + (st[2] + st[3])) + ((st[4] + st[5]) + (st[6] + st[7]));
    float mean = t.x * (1.f / 256.f);
    float var = fmaxf(fmaf(-mean, mean, t.y * (1.f / 256.f)), 0.f);
    float rstd = __builtin_amdgcn_rsqf(var + 1e-5f);
    const u32x2 ov = {pk_bf16((v4[0] - mean) * rstd, (v4[1] - mean) * rstd), pk_bf16((v4[2] - mean) * rstd, (v4[3] - mean) * rstd)};
    asm volatile("buffer_store_dwordx2 %0, %1, %2, 0 offen" ::"v"(ov), "v"(oo), "s"(rout) : "memory");
  }
}

extern "C" void kernel_launch(void* const* d_in, const int* in_sizes, int n_in, void* d_out, int out_size, void* d_ws, size_t ws_size,
                              hipStream_t stream) {
  Params p;
  p.x = (const u16*)d_in[0]; p.w1a = (const u16*)d_in[1]; p.w1b = (const u16*)d_in[2]; p.wqkv = (const u16*)d_in[3];
  p.wo = (const u16*)d_in[4]; p.wp1 = (const u16*)d_in[5]; p.dw = (const u16*)d_in[6]; p.wp2 = (const u16*)d_in[7];
  p.w2a = (const u16*)d_in[8]; p.w2b = (const u16*)d_in[9];
  p.out = (u16*)d_out; p.ws = (unsigned char*)d_ws;
  const u16* tp[8] = {p.w1a, p.w1b, p.wqkv, p.wo, p.wp1, p.wp2, p.w2a, p.w2b};
  const unsigned rs[8] = {2048, 512, 1536, 512, 1024, 512, 2048, 512}, jm[8] = {512, 131072, 128, 0, 128, 0, 512, 131072};
  const unsigned c2[8] = {128, 128, 512, 128, 512, 128, 128, 128}, cm[8] = {3, 3, 2, 3, 1, 3, 3, 3};
  for (int i = 0; i < 8; ++i) { p.tt[i].ptr = tp[i]; p.tt[i].rs = rs[i]; p.tt[i].jm = jm[i]; p.tt[i].c2 = c2[i]; p.tt[i].cm = cm[i]; p.tt[i].pad0 = 0; p.tt[i].pad1 = 0; }
  (void)hipFuncSetAttribute((const void*)conformer_kernel, hipFuncAttributeMaxDynamicSharedMemorySize, 163840);
  conformer_kernel<<<256, 512, 163840, stream>>>(p);
}
```
